```python
import torch

def _softmax(x, axis=-1):
    return torch.softmax(x, dim=axis)
def _gelu(x):
    return torch.nn.functional.gelu(x, approximate="tanh")
def _silu(x):
    return torch.nn.functional.silu(x)
def _sigmoid(x):
    return torch.sigmoid(x)
def _layernorm(x, axis=-1, eps=1e-5):
    m = x.mean(dim=axis, keepdim=True)
    v = x.var(dim=axis, keepdim=True, unbiased=False)
    return (x - m) / torch.sqrt(v + eps)
def _scatter_add(tgt, idx, src, axis=0):
    return tgt.clone().index_add_(axis, idx, src)
def _segment_sum(src, seg, n):
    out = torch.zeros((n,) + src.shape[1:], dtype=src.dtype)
    return out.index_add_(0, seg, src)
def _pad(x, pw):
    flat = tuple(v for pair in reversed(pw) for v in pair)
    return torch.nn.functional.pad(x, flat)

kernel_name = 'standalone__fused_io_aware_attn__prompt'

def setup_inputs():

    _w = (lambda a: (lambda t: t.to(torch.bfloat16) if t.is_floating_point() else t)(torch.as_tensor(a)))
    B, H, S = 2, 32, 2048
    Dh = max(1, 1024 // 32)
    return {
        "q": _w((lambda *s: torch.randn(*s, dtype=torch.bfloat16) * 0.8)(B, H, S, Dh)),
        "k": _w((lambda *s: torch.randn(*s, dtype=torch.bfloat16) * 0.8)(B, H, S, Dh)),
        "v": _w((lambda *s: torch.randn(*s, dtype=torch.bfloat16) * 0.05)(B, H, S, Dh)),
    }

def reference(q, k, v):
    scale = q.shape[-1] ** -0.5
    s = torch.einsum('bhqd,bhkd->bhqk', q, k) * scale
    p = _softmax(s, axis=-1)
    return torch.einsum('bhqk,bhkd->bhqd', p, v)
```

```cpp
#include <hip/hip_runtime.h>
#include <hip/hip_bf16.h>
#include <stdint.h>

typedef __bf16 bf16x8 __attribute__((ext_vector_type(8)));
typedef __bf16 bf16x16 __attribute__((ext_vector_type(16)));
typedef __bf16 bf16x2 __attribute__((ext_vector_type(2)));
typedef float f32x16 __attribute__((ext_vector_type(16)));
typedef float f32x8 __attribute__((ext_vector_type(8)));
typedef float f32x4 __attribute__((ext_vector_type(4)));
typedef float f32x2 __attribute__((ext_vector_type(2)));
typedef unsigned u32x4 __attribute__((ext_vector_type(4)));
typedef unsigned u32x2 __attribute__((ext_vector_type(2)));
typedef short s16x4 __attribute__((ext_vector_type(4)));
typedef short s16x8 __attribute__((ext_vector_type(8)));
typedef __attribute__((address_space(3))) const char* lds_cptr;

constexpr int NBH = 64, SEQ = 2048, DH = 32;
constexpr int NW = 8;
constexpr int QWG = 512;
constexpr int CHUNK = 256;
constexpr int NCHUNK = SEQ / CHUNK;
constexpr int NSLOT = 4;
constexpr int SLOTB = CHUNK * DH * 2 * 2;
constexpr int LDS_BYTES = NSLOT * SLOTB;
constexpr float QSCALE = 0.17677669529663687f * 1.4426950408889634f;

#define SBAR() __builtin_amdgcn_sched_barrier(0)
#define PIN(x) asm volatile("" : "+v"(x))
#define MFMA32(a, b, c) __builtin_amdgcn_mfma_f32_32x32x16_bf16(a, b, c, 0, 0, 0)
#define DI __device__ __forceinline__

DI unsigned cvtpk(float lo, float hi) { f32x2 v = {lo, hi}; bf16x2 b = __builtin_convertvector(v, bf16x2); return __builtin_bit_cast(unsigned, b); }
DI bf16x16 lo8(const f32x16& p) { f32x8 t = __builtin_shufflevector(p, p, 0, 1, 2, 3, 4, 5, 6, 7); return __builtin_bit_cast(bf16x16, t); }
DI bf16x16 hi8(const f32x16& p) { f32x8 t = __builtin_shufflevector(p, p, 8, 9, 10, 11, 12, 13, 14, 15); return __builtin_bit_cast(bf16x16, t); }
#define SM32(a, b, c) __builtin_amdgcn_smfmac_f32_32x32x32_bf16(a, b, c, sidx, 0, 0)
#define SM16(a, b, c) __builtin_amdgcn_smfmac_f32_16x16x64_bf16(a, b, c, sidx, 0, 0)

DI void glds2(unsigned voff, const void* sbase, unsigned lds_dst) {
  unsigned sv;
  asm volatile("s_mov_b32 %0, m0\n\ts_mov_b32 m0, %3\n\ts_nop 0\n\tglobal_load_lds_dwordx4 %1, %2\n\tglobal_load_lds_dwordx4 %1, %2 offset:1024\n\ts_mov_b32 m0, %0"
               : "=&s"(sv) : "v"(voff), "s"(sbase), "s"(lds_dst) : "memory");
}
DI bf16x8 ldsK(lds_cptr p) { return *(const __attribute__((address_space(3))) bf16x8*)p; }
DI s16x4 vtr(lds_cptr p) { return __builtin_amdgcn_ds_read_tr16_b64_v4i16((__attribute__((address_space(3))) s16x4*)p); }
DI bf16x8 ldsV(lds_cptr p) {
  s16x4 a = vtr(p), b = vtr(p + 256);
  s16x8 r = __builtin_shufflevector(a, b, 0, 1, 2, 3, 4, 5, 6, 7);
  return __builtin_bit_cast(bf16x8, r);
}
DI float xmax32(float m) { auto rr = __builtin_amdgcn_permlane32_swap(__float_as_uint(m), __float_as_uint(m), false, false); return fmaxf(__uint_as_float(rr[0]), __uint_as_float(rr[1])); }
DI float xsum32(float m) { auto rr = __builtin_amdgcn_permlane32_swap(__float_as_uint(m), __float_as_uint(m), false, false); return __uint_as_float(rr[0]) + __uint_as_float(rr[1]); }
DI float max16(const f32x16& s) {
  float a = fmaxf(fmaxf(s[0], s[1]), s[2]), b = fmaxf(fmaxf(s[3], s[4]), s[5]);
  a = fmaxf(fmaxf(a, s[6]), s[7]); b = fmaxf(fmaxf(b, s[8]), s[9]);
  a = fmaxf(fmaxf(a, s[10]), s[11]); b = fmaxf(fmaxf(b, s[12]), s[13]);
  a = fmaxf(fmaxf(a, s[14]), s[15]);
  return fmaxf(a, b);
}
DI bf16x8 scaleq(u32x4 w) {
  u32x4 r;
#pragma unroll
  for (int i = 0; i < 4; ++i) r[i] = cvtpk(__uint_as_float(w[i] << 16) * QSCALE, __uint_as_float(w[i] & 0xffff0000u) * QSCALE);
  return __builtin_bit_cast(bf16x8, r);
}
DI void store_tile(const f32x16& o, float rl, unsigned short* orow, int hi) {
  unsigned w[8];
#pragma unroll
  for (int i = 0; i < 8; ++i) w[i] = cvtpk(o[2 * i] * rl, o[2 * i + 1] * rl);
#pragma unroll
  for (int g = 0; g < 4; g += 2) {
    auto r0 = __builtin_amdgcn_permlane32_swap(w[2 * g], w[2 * g + 2], false, false);
    auto r1 = __builtin_amdgcn_permlane32_swap(w[2 * g + 1], w[2 * g + 3], false, false);
    u32x4 v = {r0[0], r1[0], r0[1], r1[1]};
    *(u32x4*)(orow + 8 * g + 8 * hi) = v;
  }
}

DI void slow_tile(const unsigned short* Kh, const unsigned short* Vh, bf16x8 q0, bf16x8 q1, unsigned short* orow, int lane) {
  const int r32 = lane & 31, hi = lane >> 5;
  const int sidx = (int)0xddddddddu;
  float m = -INFINITY, l = 0.f; f32x16 o = {};
  for (int t = 0; t < SEQ / 32; ++t) {
    const unsigned short* kp = Kh + (t * 32 + r32) * DH + hi * 8;
    bf16x8 k0 = *(const bf16x8*)kp, k1 = *(const bf16x8*)(kp + 16);
    f32x16 s = {};
    s = MFMA32(k0, q0, s); s = MFMA32(k1, q1, s);
    const float mn = fmaxf(m, xmax32(max16(s)));
    const float mu = (mn == -INFINITY) ? 0.f : mn;
    const float alpha = __builtin_amdgcn_exp2f(m - mu);
    m = mn;
    float ps = 0.f;
#pragma unroll
    for (int r = 0; r < 16; ++r) { float p = __builtin_amdgcn_exp2f(s[r] - mu); p = __uint_as_float(__float_as_uint(p) & 0xffff0000u); s[r] = p; ps += p; o[r] *= alpha; }
    l = l * alpha + ps;
    s16x8 v0, v1;
#pragma unroll
    for (int j = 0; j < 8; ++j) { v0[j] = (short)Vh[(t * 32 + hi * 8 + j) * DH + r32]; v1[j] = (short)Vh[(t * 32 + 16 + hi * 8 + j) * DH + r32]; }
    o = SM32(__builtin_bit_cast(bf16x8, v0), lo8(s), o);
    o = SM32(__builtin_bit_cast(bf16x8, v1), hi8(s), o);
  }
  l = xsum32(l);
  store_tile(o, 1.0f / l, orow, hi);
}

__global__ __launch_bounds__(NW * 64, 2) void attn_d32_kernel(const unsigned short* __restrict__ Q, const unsigned short* __restrict__ K,
                                                                const unsigned short* __restrict__ V, unsigned short* __restrict__ O) {
  extern __shared__ __attribute__((aligned(16))) char lds[];
  const int tid = threadIdx.x, lane = tid & 63, r32 = lane & 31, hi = lane >> 5;
  const int wid = __builtin_amdgcn_readfirstlane(tid >> 6);
  const int bid = blockIdx.x, bh = (bid & 7) * 8 + (bid >> 5), qb = (bid >> 3) & 3;
  const unsigned short* Kh = K + (size_t)bh * SEQ * DH;
  const unsigned short* Vh = V + (size_t)bh * SEQ * DH;
  const int q0 = qb * QWG + wid * 64;
  const unsigned short* Qw = Q + ((size_t)bh * SEQ + q0 + r32) * DH + hi * 8;
  const u32x4 qa0 = *(const u32x4*)(Qw), qa1 = *(const u32x4*)(Qw + 16), qb0 = *(const u32x4*)(Qw + 32 * DH), qb1 = *(const u32x4*)(Qw + 32 * DH + 16);
  const unsigned lds0 = (unsigned)(uintptr_t)lds;
  const unsigned koff = wid * 2048 + (lane >> 2) * 64 + (((lane & 3) ^ ((lane >> 4) & 3)) * 16), voff = wid * 2048 + lane * 16;
#define DMA(c) do { glds2(koff, (const char*)Kh + (c) * (CHUNK * 64), lds0 + ((c) % NSLOT) * SLOTB + wid * 2048); \
                    glds2(voff, (const char*)Vh + (c) * (CHUNK * 64), lds0 + ((c) % NSLOT) * SLOTB + SLOTB / 2 + wid * 2048); } while (0)
  DMA(0); DMA(1); DMA(2);
  const bf16x8 qA0 = scaleq(qa0), qA1 = scaleq(qa1), qB0 = scaleq(qb0), qB1 = scaleq(qb1);
  const int sidx = (int)0xddddddddu;
  u32x4 onesw; { const unsigned o1 = 0x3f803f80u; const bool up = (lane >> 2) & 1; onesw = up ? u32x4{0u, 0u, o1, o1} : u32x4{o1, o1, 0u, 0u}; }
  const bf16x8 ones = __builtin_bit_cast(bf16x8, onesw);
  const int ksw = (r32 >> 2) & 3;
  const lds_cptr kp0 = (lds_cptr)lds + r32 * 64 + ((hi ^ ksw) * 16), kp1 = (lds_cptr)lds + r32 * 64 + (((2 + hi) ^ ksw) * 16);
  const lds_cptr vp = (lds_cptr)lds + SLOTB / 2 + (8 * hi + ((lane & 15) >> 2)) * 64 + ((lane >> 4) & 1) * 32 + (lane & 3) * 8;
  asm volatile("s_waitcnt vmcnt(8)\n\ts_barrier" ::: "memory");
  bf16x8 k0 = ldsK(kp0), k1 = ldsK(kp1);
  f32x16 nmA, nmB, sA, sB;
  {
    f32x16 z = {};
    sA = MFMA32(k0, qA0, z); sA = MFMA32(k1, qA1, sA);
    sB = MFMA32(k0, qB0, z); sB = MFMA32(k1, qB1, sB);
    const float mA = -xmax32(max16(sA)), mB = -xmax32(max16(sB));
#pragma unroll
    for (int r = 0; r < 16; ++r) { nmA[r] = mA; nmB[r] = mB; }
  }
  PIN(nmA); PIN(nmB);
  f32x16 oA = {}, oB = {}; f32x4 lA = {}, lB = {};
  sA = MFMA32(k0, qA0, nmA); sA = MFMA32(k1, qA1, sA);
  sB = f32x16{};
  bf16x8 vlo = ldsV(vp), vhi = ldsV(vp);
  bf16x8 kn0, kn1, vhn;
#define EX(S, i) S[i] = __builtin_amdgcn_exp2f(S[i])
#define SLOT(X, Y, oX, lX, oY, lY, nmY, qY0, qY1, KA, KB, VH, VL, LD0, LD1, LD2) do { \
    oY = SM32(VH, hi8(Y), oY); LD0; EX(X, 0); EX(X, 1); EX(X, 2); PIN(X); SBAR(); \
    lY = SM16(ones, hi8(Y), lY); EX(X, 3); EX(X, 4); PIN(X); SBAR(); \
    Y = MFMA32(KA, qY0, nmY); LD1; EX(X, 5); EX(X, 6); EX(X, 7); PIN(X); SBAR(); \
    Y = MFMA32(KB, qY1, Y); LD2; EX(X, 8); EX(X, 9); EX(X, 10); PIN(X); SBAR(); \
    oX = SM32(VL, lo8(X), oX); EX(X, 11); EX(X, 12); EX(X, 13); PIN(X); SBAR(); \
    lX = SM16(ones, lo8(X), lX); EX(X, 14); EX(X, 15); PIN(X); SBAR(); } while (0)
#define TILE(OFF, NOFF) do { \
    SLOT(sA, sB, oA, lA, oB, lB, nmB, qB0, qB1, k0, k1, vhi, vlo, (vhn = ldsV(vp + (OFF) + 1024)), (kn0 = ldsK(kp0 + (NOFF))), (kn1 = ldsK(kp1 + (NOFF)))); \
    SLOT(sB, sA, oB, lB, oA, lA, nmA, qA0, qA1, kn0, kn1, vhn, vlo, (void)0, (void)0, (void)0); \
    k0 = kn0; k1 = kn1; vhi = vhn; vlo = ldsV(vp + (NOFF)); } while (0)
  for (int c = 0; c < NCHUNK; ++c) {
    const int sl = (c % NSLOT) * SLOTB, sn = ((c + 1) % NSLOT) * SLOTB;
    TILE(sl, sl + 2048); TILE(sl + 2048, sl + 4096); TILE(sl + 4096, sl + 6144); TILE(sl + 6144, sl + 8192);
    if (c + 2 < NCHUNK) asm volatile("s_waitcnt vmcnt(4)\n\ts_barrier" ::: "memory"); else asm volatile("s_waitcnt vmcnt(0)\n\ts_barrier" ::: "memory");
    if (c + 3 < NCHUNK) DMA(c + 3);
    TILE(sl + 8192, sl + 10240); TILE(sl + 10240, sl + 12288); TILE(sl + 12288, sl + 14336); TILE(sl + 14336, sn);
  }
  oB = SM32(vhi, hi8(sB), oB); lB = SM16(ones, hi8(sB), lB);
  float chk = 0.f;
#pragma unroll
  for (int r = 0; r < 16; ++r) chk += fabsf(oA[r]) + fabsf(oB[r]);
  const bool ok = (lA[0] <= 1.8446744e19f) && (lB[0] <= 1.8446744e19f) && (chk <= 3.0e38f) && (lA[0] >= 0.25f) && (lB[0] >= 0.25f);
  unsigned short* Ow = O + ((size_t)bh * SEQ + q0 + r32) * DH;
  if (__builtin_expect(__all(ok), 1)) {
    store_tile(oA, __builtin_amdgcn_rcpf(lA[0]), Ow, hi);
    store_tile(oB, __builtin_amdgcn_rcpf(lB[0]), Ow + 32 * DH, hi);
  } else {
#pragma unroll 1
    for (int u = 0; u < 2; ++u) slow_tile(Kh, Vh, u ? qB0 : qA0, u ? qB1 : qA1, Ow + u * 32 * DH, lane);
  }
}

extern "C" void kernel_launch(void* const* d_in, const int* in_sizes, int n_in, void* d_out, int out_size, void* d_ws, size_t ws_size, hipStream_t stream) {
  (void)in_sizes; (void)n_in; (void)out_size; (void)d_ws; (void)ws_size;
  (void)hipFuncSetAttribute((const void*)attn_d32_kernel, hipFuncAttributeMaxDynamicSharedMemorySize, LDS_BYTES);
  hipLaunchKernelGGL(attn_d32_kernel, dim3(NBH * (SEQ / QWG)), dim3(NW * 64), LDS_BYTES, stream,
                     (const unsigned short*)d_in[0], (const unsigned short*)d_in[1], (const unsigned short*)d_in[2], (unsigned short*)d_out);
}
```

```cpp
#include <hip/hip_runtime.h>
#include <hip/hip_bf16.h>
#include <stdint.h>

typedef __bf16 bf16x8 __attribute__((ext_vector_type(8)));
typedef __bf16 bf16x16 __attribute__((ext_vector_type(16)));
typedef __bf16 bf16x2 __attribute__((ext_vector_type(2)));
typedef float f32x16 __attribute__((ext_vector_type(16)));
typedef float f32x8 __attribute__((ext_vector_type(8)));
typedef float f32x4 __attribute__((ext_vector_type(4)));
typedef float f32x2 __attribute__((ext_vector_type(2)));
typedef unsigned u32x4 __attribute__((ext_vector_type(4)));
typedef unsigned u32x2 __attribute__((ext_vector_type(2)));
typedef short s16x4 __attribute__((ext_vector_type(4)));
typedef short s16x8 __attribute__((ext_vector_type(8)));
typedef __attribute__((address_space(3))) const char* lds_cptr;

constexpr int NBH = 64, SEQ = 2048, DH = 32;
constexpr int NW = 8;
constexpr int QWG = 512;
constexpr int CHUNK = 256;
constexpr int NCHUNK = SEQ / CHUNK;
constexpr int NSLOT = 4;
constexpr int SLOTB = CHUNK * DH * 2 * 2;
constexpr int LDS_CNT = NSLOT * SLOTB;
constexpr int LDS_BYTES = LDS_CNT + 64;
constexpr float QSCALE = 0.17677669529663687f * 1.4426950408889634f;

#define SBAR() __builtin_amdgcn_sched_barrier(0)
#define PIN(x) asm volatile("" : "+v"(x))
#define MFMA32(a, b, c) __builtin_amdgcn_mfma_f32_32x32x16_bf16(a, b, c, 0, 0, 0)
#define DI __device__ __forceinline__
#define STAMP(i) do {} while (0)

DI unsigned cvtpk(float lo, float hi) { f32x2 v = {lo, hi}; bf16x2 b = __builtin_convertvector(v, bf16x2); return __builtin_bit_cast(unsigned, b); }
DI bf16x16 lo8(const f32x16& p) { f32x8 t = __builtin_shufflevector(p, p, 0, 1, 2, 3, 4, 5, 6, 7); return __builtin_bit_cast(bf16x16, t); }
DI bf16x16 hi8(const f32x16& p) { f32x8 t = __builtin_shufflevector(p, p, 8, 9, 10, 11, 12, 13, 14, 15); return __builtin_bit_cast(bf16x16, t); }
#define SM32(a, b, c) __builtin_amdgcn_smfmac_f32_32x32x32_bf16(a, b, c, sidx, 0, 0)
#define SM16(a, b, c) __builtin_amdgcn_smfmac_f32_16x16x64_bf16(a, b, c, sidx, 0, 0)

DI void glds2(unsigned voff, const void* sbase, unsigned lds_dst) {
  unsigned sv;
  asm volatile("s_mov_b32 %0, m0\n\ts_mov_b32 m0, %3\n\ts_nop 0\n\tglobal_load_lds_dwordx4 %1, %2\n\tglobal_load_lds_dwordx4 %1, %2 offset:1024\n\ts_mov_b32 m0, %0"
               : "=&s"(sv) : "v"(voff), "s"(sbase), "s"(lds_dst) : "memory");
}
DI void glds1(unsigned voff, const void* sbase, unsigned lds_dst) {
  unsigned sv;
  asm volatile("s_mov_b32 %0, m0\n\ts_mov_b32 m0, %3\n\ts_nop 0\n\tglobal_load_lds_dwordx4 %1, %2\n\ts_mov_b32 m0, %0"
               : "=&s"(sv) : "v"(voff), "s"(sbase), "s"(lds_dst) : "memory");
}
DI u32x4 gload16(const void* p) { u32x4 r; asm volatile("global_load_dwordx4 %0, %1, off" : "=v"(r) : "v"(p) : "memory"); return r; }
DI bf16x8 ldsK(lds_cptr p) { return *(const __attribute__((address_space(3))) bf16x8*)p; }
DI s16x4 vtr(lds_cptr p) { return __builtin_amdgcn_ds_read_tr16_b64_v4i16((__attribute__((address_space(3))) s16x4*)p); }
DI bf16x8 ldsV(lds_cptr p) {
  s16x4 a = vtr(p), b = vtr(p + 256);
  s16x8 r = __builtin_shufflevector(a, b, 0, 1, 2, 3, 4, 5, 6, 7);
  return __builtin_bit_cast(bf16x8, r);
}
DI float xmax32(float m) { auto rr = __builtin_amdgcn_permlane32_swap(__float_as_uint(m), __float_as_uint(m), false, false); return fmaxf(__uint_as_float(rr[0]), __uint_as_float(rr[1])); }
DI float xsum32(float m) { auto rr = __builtin_amdgcn_permlane32_swap(__float_as_uint(m), __float_as_uint(m), false, false); return __uint_as_float(rr[0]) + __uint_as_float(rr[1]); }
DI float max16(const f32x16& s) {
  float a = fmaxf(fmaxf(s[0], s[1]), s[2]), b = fmaxf(fmaxf(s[3], s[4]), s[5]);
  a = fmaxf(fmaxf(a, s[6]), s[7]); b = fmaxf(fmaxf(b, s[8]), s[9]);
  a = fmaxf(fmaxf(a, s[10]), s[11]); b = fmaxf(fmaxf(b, s[12]), s[13]);
  a = fmaxf(fmaxf(a, s[14]), s[15]);
  return fmaxf(a, b);
}
DI bf16x8 scaleq(u32x4 w) {
  u32x4 r;
#pragma unroll
  for (int i = 0; i < 4; ++i) r[i] = cvtpk(__uint_as_float(w[i] << 16) * QSCALE, __uint_as_float(w[i] & 0xffff0000u) * QSCALE);
  return __builtin_bit_cast(bf16x8, r);
}
DI void store_tile(const f32x16& o, float rl, unsigned short* orow, int hi) {
  unsigned w[8];
#pragma unroll
  for (int i = 0; i < 8; ++i) w[i] = cvtpk(o[2 * i] * rl, o[2 * i + 1] * rl);
#pragma unroll
  for (int g = 0; g < 4; g += 2) {
    auto r0 = __builtin_amdgcn_permlane32_swap(w[2 * g], w[2 * g + 2], false, false);
    auto r1 = __builtin_amdgcn_permlane32_swap(w[2 * g + 1], w[2 * g + 3], false, false);
    u32x4 v = {r0[0], r1[0], r0[1], r1[1]};
    *(u32x4*)(orow + 8 * g + 8 * hi) = v;
  }
}

DI void slow_tile(const unsigned short* Kh, const unsigned short* Vh, bf16x8 q0, bf16x8 q1, unsigned short* orow, int lane) {
  const int r32 = lane & 31, hi = lane >> 5;
  const int sidx = (int)0xddddddddu;
  float m = -INFINITY, l = 0.f; f32x16 o = {};
  for (int t = 0; t < SEQ / 32; ++t) {
    const unsigned short* kp = Kh + (t * 32 + r32) * DH + hi * 8;
    bf16x8 k0 = *(const bf16x8*)kp, k1 = *(const bf16x8*)(kp + 16);
    f32x16 s = {};
    s = MFMA32(k0, q0, s); s = MFMA32(k1, q1, s);
    const float mn = fmaxf(m, xmax32(max16(s)));
    const float mu = (mn == -INFINITY) ? 0.f : mn;
    const float alpha = __builtin_amdgcn_exp2f(m - mu);
    m = mn;
    float ps = 0.f;
#pragma unroll
    for (int r = 0; r < 16; ++r) { float p = __builtin_amdgcn_exp2f(s[r] - mu); p = __uint_as_float(__float_as_uint(p) & 0xffff0000u); s[r] = p; ps += p; o[r] *= alpha; }
    l = l * alpha + ps;
    s16x8 v0, v1;
#pragma unroll
    for (int j = 0; j < 8; ++j) { v0[j] = (short)Vh[(t * 32 + hi * 8 + j) * DH + r32]; v1[j] = (short)Vh[(t * 32 + 16 + hi * 8 + j) * DH + r32]; }
    o = SM32(__builtin_bit_cast(bf16x8, v0), lo8(s), o);
    o = SM32(__builtin_bit_cast(bf16x8, v1), hi8(s), o);
  }
  l = xsum32(l);
  store_tile(o, 1.0f / l, orow, hi);
}

__global__ __launch_bounds__(NW * 64, 2) void attn_d32_kernel(const unsigned short* __restrict__ Q, const unsigned short* __restrict__ K,
                                                                const unsigned short* __restrict__ V, unsigned short* __restrict__ O, unsigned long long* __restrict__ stamps) {
  extern __shared__ __attribute__((aligned(16))) char lds[];
  STAMP(0);
  const int tid = threadIdx.x, lane = tid & 63, r32 = lane & 31, hi = lane >> 5;
  const int wid = __builtin_amdgcn_readfirstlane(tid >> 6);
  const int bid = blockIdx.x, bh = (bid & 7) * 8 + (bid >> 5), qb = (bid >> 3) & 3;
  const unsigned short* Kh = K + (size_t)bh * SEQ * DH;
  const unsigned short* Vh = V + (size_t)bh * SEQ * DH;
  const int q0 = qb * QWG + wid * 64;
  const unsigned short* Qw = Q + ((size_t)bh * SEQ + q0 + r32) * DH + hi * 8;
  u32x4 qa0 = gload16(Qw), qa1 = gload16(Qw + 16), qb0 = gload16(Qw + 32 * DH), qb1 = gload16(Qw + 32 * DH + 16);
  const unsigned lds0 = (unsigned)(uintptr_t)(lds_cptr)lds;
  const unsigned kin = (lane >> 2) * 64 + (((lane & 3) ^ ((lane >> 4) & 3)) * 16), vin = lane * 16;
  const unsigned koff = wid * 2048 + kin, voff = wid * 2048 + vin;
#define DMA(c) do { glds2(koff, (const char*)Kh + (c) * (CHUNK * 64), lds0 + ((c) % NSLOT) * SLOTB + wid * 2048); \
                    glds2(voff, (const char*)Vh + (c) * (CHUNK * 64), lds0 + ((c) % NSLOT) * SLOTB + SLOTB / 2 + wid * 2048); } while (0)
  {
    const bool isv = wid >= 4; const int p = wid & 3;
    const char* src = isv ? (const char*)Vh : (const char*)Kh; const unsigned in = isv ? vin : kin, dst = lds0 + (isv ? SLOTB / 2 : 0);
    glds1(in + p * 1024, src, dst + p * 1024);
    glds1(in + (4 + p) * 1024, src, dst + (4 + p) * 1024);
    glds1(in + (8 + p) * 1024, src, dst + (8 + p) * 1024);
    glds1(in + (12 + p) * 1024, src, dst + (12 + p) * 1024);
  }
  DMA(1);
  if (tid == 0) *(volatile __attribute__((address_space(3))) unsigned*)((__attribute__((address_space(3))) char*)lds + LDS_CNT) = 0u;
  asm volatile("s_waitcnt vmcnt(8)" : "+v"(qa0), "+v"(qa1), "+v"(qb0), "+v"(qb1) :: "memory");
  const bf16x8 qA0 = scaleq(qa0), qA1 = scaleq(qa1), qB0 = scaleq(qb0), qB1 = scaleq(qb1);
  const int sidx = (int)0xddddddddu;
  u32x4 onesw; { const unsigned o1 = 0x3f803f80u; const bool up = (lane >> 2) & 1; onesw = up ? u32x4{0u, 0u, o1, o1} : u32x4{o1, o1, 0u, 0u}; if (lane & 11) onesw = u32x4{0u, 0u, 0u, 0u}; }
  const bf16x8 ones = __builtin_bit_cast(bf16x8, onesw);
  const int ksw = (r32 >> 2) & 3;
  const lds_cptr kp0 = (lds_cptr)lds + r32 * 64 + ((hi ^ ksw) * 16), kp1 = (lds_cptr)lds + r32 * 64 + (((2 + hi) ^ ksw) * 16);
  const lds_cptr vp = (lds_cptr)lds + SLOTB / 2 + (8 * hi + ((lane & 15) >> 2)) * 64 + ((lane >> 4) & 1) * 32 + (lane & 3) * 8;
  asm volatile("s_waitcnt vmcnt(7) lgkmcnt(0)\n\ts_barrier" ::: "memory");
  STAMP(1);
  bf16x8 k0 = ldsK(kp0), k1 = ldsK(kp1);
  f32x16 nmA, nmB, sA, sB;
  {
    f32x16 z = {};
    sA = MFMA32(k0, qA0, z); sA = MFMA32(k1, qA1, sA);
    sB = MFMA32(k0, qB0, z); sB = MFMA32(k1, qB1, sB);
    const float mA = -xmax32(max16(sA)), mB = -xmax32(max16(sB));
#pragma unroll
    for (int r = 0; r < 16; ++r) { nmA[r] = mA; nmB[r] = mB; }
  }
  f32x16 oA = {}, oB = {}; f32x4 lA = {}, lB = {};
  sA = MFMA32(k0, qA0, nmA); sA = MFMA32(k1, qA1, sA);
  sB = f32x16{};
  bf16x8 vl = ldsV(vp), vh = ldsV(vp + 1024), vhz = {};
  unsigned kp0u = (unsigned)(uintptr_t)kp0, kp1u = (unsigned)(uintptr_t)kp1, vpu = (unsigned)(uintptr_t)vp;
  const unsigned cnta = lds0 + LDS_CNT, dst0 = lds0 + wid * 2048;
  STAMP(2);
  asm volatile(
      "s_nop 7\n\t"
      "s_nop 7\n\t"
      "s_mov_b64 s[60:61], %[kptr]\n\t"
      "s_mov_b64 s[62:63], %[vptr]\n\t"
      "s_mov_b32 s64, 0\n\t"
      "s_mov_b32 s67, 8\n\t"
      ".p2align 6\n\t"
      "1:\n\t"
      "v_exp_f32 v64, v64\n\t"
      "v_exp_f32 v65, v65\n\t"
      "v_smfmac_f32_32x32x32_bf16 v[16:31], v[152:155], v[88:95], v156\n\t"
      "v_exp_f32 v66, v66\n\t"
      "v_exp_f32 v67, v67\n\t"
      "v_exp_f32 v68, v68\n\t"
      "v_smfmac_f32_16x16x64_bf16 v[116:119], v[120:123], v[88:95], v156\n\t"
      "ds_read_b128 v[132:135], v157 offset:2048\n\t"
      "v_exp_f32 v69, v69\n\t"
      "v_exp_f32 v70, v70\n\t"
      "v_mfma_f32_32x32x16_bf16 v[80:95], v[124:127], v[104:107], v[48:63]\n\t"
      "v_exp_f32 v71, v71\n\t"
      "v_exp_f32 v72, v72\n\t"
      "v_exp_f32 v73, v73\n\t"
      "v_mfma_f32_32x32x16_bf16 v[80:95], v[128:131], v[108:111], v[80:95]\n\t"
      "s_waitcnt lgkmcnt(3)\n\t"
      "v_exp_f32 v74, v74\n\t"
      "v_exp_f32 v75, v75\n\t"
      "v_exp_f32 v76, v76\n\t"
      "v_smfmac_f32_32x32x32_bf16 v[0:15], v[140:143], v[64:71], v156\n\t"
      "s_nop 0\n\t"
      "ds_read_b128 v[136:139], v158 offset:2048\n\t"
      "v_exp_f32 v77, v77\n\t"
      "v_exp_f32 v78, v78\n\t"
      "v_exp_f32 v79, v79\n\t"
      "v_smfmac_f32_16x16x64_bf16 v[112:115], v[120:123], v[64:71], v156\n\t"
      "s_waitcnt lgkmcnt(2)\n\t"
      "ds_read_b64_tr_b16 v[148:149], v159 offset:2048\n\t"
      "v_exp_f32 v80, v80\n\t"
      "v_exp_f32 v81, v81\n\t"
      "v_smfmac_f32_32x32x32_bf16 v[0:15], v[144:147], v[72:79], v156\n\t"
      "v_exp_f32 v82, v82\n\t"
      "v_exp_f32 v83, v83\n\t"
      "v_exp_f32 v84, v84\n\t"
      "v_smfmac_f32_16x16x64_bf16 v[112:115], v[120:123], v[72:79], v156\n\t"
      "s_waitcnt lgkmcnt(2)\n\t"
      "ds_read_b64_tr_b16 v[150:151], v159 offset:2304\n\t"
      "v_exp_f32 v85, v85\n\t"
      "v_exp_f32 v86, v86\n\t"
      "v_mfma_f32_32x32x16_bf16 v[64:79], v[132:135], v[96:99], v[32:47]\n\t"
      "s_waitcnt lgkmcnt(2)\n\t"
      "v_exp_f32 v87, v87\n\t"
      "v_exp_f32 v88, v88\n\t"
      "v_exp_f32 v89, v89\n\t"
      "v_mfma_f32_32x32x16_bf16 v[64:79], v[136:139], v[100:103], v[64:79]\n\t"
      "s_nop 0\n\t"
      "v_exp_f32 v90, v90\n\t"
      "v_exp_f32 v91, v91\n\t"
      "v_exp_f32 v92, v92\n\t"
      "v_smfmac_f32_32x32x32_bf16 v[16:31], v[140:143], v[80:87], v156\n\t"
      "s_nop 0\n\t"
      "ds_read_b64_tr_b16 v[152:153], v159 offset:3072\n\t"
      "v_exp_f32 v93, v93\n\t"
      "v_exp_f32 v94, v94\n\t"
      "v_exp_f32 v95, v95\n\t"
      "v_smfmac_f32_16x16x64_bf16 v[116:119], v[120:123], v[80:87], v156\n\t"
      "s_nop 0\n\t"
      "ds_read_b64_tr_b16 v[154:155], v159 offset:3328\n\t"
      "s_cmp_lg_u32 s64, 0\n\t"
      "s_cbranch_scc1 2f\n\t"
      "s_waitcnt vmcnt(4)\n\t"
      "s_barrier\n\t"
      "s_add_u32 m0, %[dst0], 0x10000\n\t"
      "s_nop 0\n\t"
      "global_load_lds_dwordx4 v160, s[60:61]\n\t"
      "global_load_lds_dwordx4 v160, s[60:61] offset:1024\n\t"
      "s_add_u32 m0, %[dst0], 0x14000\n\t"
      "s_nop 0\n\t"
      "global_load_lds_dwordx4 v161, s[62:63]\n\t"
      "global_load_lds_dwordx4 v161, s[62:63] offset:1024\n\t"
      "s_add_u32 s60, s60, 0x4000\n\t"
      "s_addc_u32 s61, s61, 0\n\t"
      "s_add_u32 s62, s62, 0x4000\n\t"
      "s_addc_u32 s63, s63, 0\n\t"
      "2:\n\t"
      "v_exp_f32 v64, v64\n\t"
      "v_exp_f32 v65, v65\n\t"
      "v_smfmac_f32_32x32x32_bf16 v[16:31], v[144:147], v[88:95], v156\n\t"
      "v_exp_f32 v66, v66\n\t"
      "v_exp_f32 v67, v67\n\t"
      "v_exp_f32 v68, v68\n\t"
      "v_smfmac_f32_16x16x64_bf16 v[116:119], v[120:123], v[88:95], v156\n\t"
      "ds_read_b128 v[124:127], v157 offset:4096\n\t"
      "v_exp_f32 v69, v69\n\t"
      "v_exp_f32 v70, v70\n\t"
      "v_mfma_f32_32x32x16_bf16 v[80:95], v[132:135], v[104:107], v[48:63]\n\t"
      "v_exp_f32 v71, v71\n\t"
      "v_exp_f32 v72, v72\n\t"
      "v_exp_f32 v73, v73\n\t"
      "v_mfma_f32_32x32x16_bf16 v[80:95], v[136:139], v[108:111], v[80:95]\n\t"
      "s_waitcnt lgkmcnt(3)\n\t"
      "v_exp_f32 v74, v74\n\t"
      "v_exp_f32 v75, v75\n\t"
      "v_exp_f32 v76, v76\n\t"
      "v_smfmac_f32_32x32x32_bf16 v[0:15], v[148:151], v[64:71], v156\n\t"
      "s_nop 0\n\t"
      "ds_read_b128 v[128:131], v158 offset:4096\n\t"
      "v_exp_f32 v77, v77\n\t"
      "v_exp_f32 v78, v78\n\t"
      "v_exp_f32 v79, v79\n\t"
      "v_smfmac_f32_16x16x64_bf16 v[112:115], v[120:123], v[64:71], v156\n\t"
      "s_waitcnt lgkmcnt(2)\n\t"
      "ds_read_b64_tr_b16 v[140:141], v159 offset:4096\n\t"
      "v_exp_f32 v80, v80\n\t"
      "v_exp_f32 v81, v81\n\t"
      "v_smfmac_f32_32x32x32_bf16 v[0:15], v[152:155], v[72:79], v156\n\t"
      "v_exp_f32 v82, v82\n\t"
      "v_exp_f32 v83, v83\n\t"
      "v_exp_f32 v84, v84\n\t"
      "v_smfmac_f32_16x16x64_bf16 v[112:115], v[120:123], v[72:79], v156\n\t"
      "s_waitcnt lgkmcnt(2)\n\t"
      "ds_read_b64_tr_b16 v[142:143], v159 offset:4352\n\t"
      "v_exp_f32 v85, v85\n\t"
      "v_exp_f32 v86, v86\n\t"
      "v_mfma_f32_32x32x16_bf16 v[64:79], v[124:127], v[96:99], v[32:47]\n\t"
      "s_waitcnt lgkmcnt(2)\n\t"
      "v_exp_f32 v87, v87\n\t"
      "v_exp_f32 v88, v88\n\t"
      "v_exp_f32 v89, v89\n\t"
      "v_mfma_f32_32x32x16_bf16 v[64:79], v[128:131], v[100:103], v[64:79]\n\t"
      "s_nop 0\n\t"
      "v_exp_f32 v90, v90\n\t"
      "v_exp_f32 v91, v91\n\t"
      "v_exp_f32 v92, v92\n\t"
      "v_smfmac_f32_32x32x32_bf16 v[16:31], v[148:151], v[80:87], v156\n\t"
      "s_nop 0\n\t"
      "ds_read_b64_tr_b16 v[144:145], v159 offset:5120\n\t"
      "v_exp_f32 v93, v93\n\t"
      "v_exp_f32 v94, v94\n\t"
      "v_exp_f32 v95, v95\n\t"
      "v_smfmac_f32_16x16x64_bf16 v[116:119], v[120:123], v[80:87], v156\n\t"
      "s_nop 0\n\t"
      "ds_read_b64_tr_b16 v[146:147], v159 offset:5376\n\t"
      "v_exp_f32 v64, v64\n\t"
      "v_exp_f32 v65, v65\n\t"
      "v_smfmac_f32_32x32x32_bf16 v[16:31], v[152:155], v[88:95], v156\n\t"
      "v_exp_f32 v66, v66\n\t"
      "v_exp_f32 v67, v67\n\t"
      "v_exp_f32 v68, v68\n\t"
      "v_smfmac_f32_16x16x64_bf16 v[116:119], v[120:123], v[88:95], v156\n\t"
      "ds_read_b128 v[132:135], v157 offset:6144\n\t"
      "v_exp_f32 v69, v69\n\t"
      "v_exp_f32 v70, v70\n\t"
      "v_mfma_f32_32x32x16_bf16 v[80:95], v[124:127], v[104:107], v[48:63]\n\t"
      "v_exp_f32 v71, v71\n\t"
      "v_exp_f32 v72, v72\n\t"
      "v_exp_f32 v73, v73\n\t"
      "v_mfma_f32_32x32x16_bf16 v[80:95], v[128:131], v[108:111], v[80:95]\n\t"
      "s_waitcnt lgkmcnt(3)\n\t"
      "v_exp_f32 v74, v74\n\t"
      "v_exp_f32 v75, v75\n\t"
      "v_exp_f32 v76, v76\n\t"
      "v_smfmac_f32_32x32x32_bf16 v[0:15], v[140:143], v[64:71], v156\n\t"
      "s_nop 0\n\t"
      "ds_read_b128 v[136:139], v158 offset:6144\n\t"
      "v_exp_f32 v77, v77\n\t"
      "v_exp_f32 v78, v78\n\t"
      "v_exp_f32 v79, v79\n\t"
      "v_smfmac_f32_16x16x64_bf16 v[112:115], v[120:123], v[64:71], v156\n\t"
      "s_waitcnt lgkmcnt(2)\n\t"
      "ds_read_b64_tr_b16 v[148:149], v159 offset:6144\n\t"
      "v_exp_f32 v80, v80\n\t"
      "v_exp_f32 v81, v81\n\t"
      "v_smfmac_f32_32x32x32_bf16 v[0:15], v[144:147], v[72:79], v156\n\t"
      "v_exp_f32 v82, v82\n\t"
      "v_exp_f32 v83, v83\n\t"
      "v_exp_f32 v84, v84\n\t"
      "v_smfmac_f32_16x16x64_bf16 v[112:115], v[120:123], v[72:79], v156\n\t"
      "s_waitcnt lgkmcnt(2)\n\t"
      "ds_read_b64_tr_b16 v[150:151], v159 offset:6400\n\t"
      "v_exp_f32 v85, v85\n\t"
      "v_exp_f32 v86, v86\n\t"
      "v_mfma_f32_32x32x16_bf16 v[64:79], v[132:135], v[96:99], v[32:47]\n\t"
      "s_waitcnt lgkmcnt(2)\n\t"
      "v_exp_f32 v87, v87\n\t"
      "v_exp_f32 v88, v88\n\t"
      "v_exp_f32 v89, v89\n\t"
      "v_mfma_f32_32x32x16_bf16 v[64:79], v[136:139], v[100:103], v[64:79]\n\t"
      "s_nop 0\n\t"
      "v_exp_f32 v90, v90\n\t"
      "v_exp_f32 v91, v91\n\t"
      "v_exp_f32 v92, v92\n\t"
      "v_smfmac_f32_32x32x32_bf16 v[16:31], v[140:143], v[80:87], v156\n\t"
      "s_nop 0\n\t"
      "ds_read_b64_tr_b16 v[152:153], v159 offset:7168\n\t"
      "v_exp_f32 v93, v93\n\t"
      "v_exp_f32 v94, v94\n\t"
      "v_exp_f32 v95, v95\n\t"
      "v_smfmac_f32_16x16x64_bf16 v[116:119], v[120:123], v[80:87], v156\n\t"
      "s_nop 0\n\t"
      "ds_read_b64_tr_b16 v[154:155], v159 offset:7424\n\t"
      "v_exp_f32 v64, v64\n\t"
      "v_exp_f32 v65, v65\n\t"
      "v_smfmac_f32_32x32x32_bf16 v[16:31], v[144:147], v[88:95], v156\n\t"
      "v_exp_f32 v66, v66\n\t"
      "v_exp_f32 v67, v67\n\t"
      "v_exp_f32 v68, v68\n\t"
      "v_smfmac_f32_16x16x64_bf16 v[116:119], v[120:123], v[88:95], v156\n\t"
      "ds_read_b128 v[124:127], v157 offset:8192\n\t"
      "v_exp_f32 v69, v69\n\t"
      "v_exp_f32 v70, v70\n\t"
      "v_mfma_f32_32x32x16_bf16 v[80:95], v[132:135], v[104:107], v[48:63]\n\t"
      "v_exp_f32 v71, v71\n\t"
      "v_exp_f32 v72, v72\n\t"
      "v_exp_f32 v73, v73\n\t"
      "v_mfma_f32_32x32x16_bf16 v[80:95], v[136:139], v[108:111], v[80:95]\n\t"
      "s_waitcnt lgkmcnt(3)\n\t"
      "v_exp_f32 v74, v74\n\t"
      "v_exp_f32 v75, v75\n\t"
      "v_exp_f32 v76, v76\n\t"
      "v_smfmac_f32_32x32x32_bf16 v[0:15], v[148:151], v[64:71], v156\n\t"
      "s_nop 0\n\t"
      "ds_read_b128 v[128:131], v158 offset:8192\n\t"
      "v_exp_f32 v77, v77\n\t"
      "v_exp_f32 v78, v78\n\t"
      "v_exp_f32 v79, v79\n\t"
      "v_smfmac_f32_16x16x64_bf16 v[112:115], v[120:123], v[64:71], v156\n\t"
      "s_waitcnt lgkmcnt(2)\n\t"
      "ds_read_b64_tr_b16 v[140:141], v159 offset:8192\n\t"
      "v_exp_f32 v80, v80\n\t"
      "v_exp_f32 v81, v81\n\t"
      "v_smfmac_f32_32x32x32_bf16 v[0:15], v[152:155], v[72:79], v156\n\t"
      "v_exp_f32 v82, v82\n\t"
      "v_exp_f32 v83, v83\n\t"
      "v_exp_f32 v84, v84\n\t"
      "v_smfmac_f32_16x16x64_bf16 v[112:115], v[120:123], v[72:79], v156\n\t"
      "s_waitcnt lgkmcnt(2)\n\t"
      "ds_read_b64_tr_b16 v[142:143], v159 offset:8448\n\t"
      "v_exp_f32 v85, v85\n\t"
      "v_exp_f32 v86, v86\n\t"
      "v_mfma_f32_32x32x16_bf16 v[64:79], v[124:127], v[96:99], v[32:47]\n\t"
      "s_waitcnt lgkmcnt(2)\n\t"
      "v_exp_f32 v87, v87\n\t"
      "v_exp_f32 v88, v88\n\t"
      "v_exp_f32 v89, v89\n\t"
      "v_mfma_f32_32x32x16_bf16 v[64:79], v[128:131], v[100:103], v[64:79]\n\t"
      "s_nop 0\n\t"
      "v_exp_f32 v90, v90\n\t"
      "v_exp_f32 v91, v91\n\t"
      "v_exp_f32 v92, v92\n\t"
      "v_smfmac_f32_32x32x32_bf16 v[16:31], v[148:151], v[80:87], v156\n\t"
      "s_nop 0\n\t"
      "ds_read_b64_tr_b16 v[144:145], v159 offset:9216\n\t"
      "v_exp_f32 v93, v93\n\t"
      "v_exp_f32 v94, v94\n\t"
      "v_exp_f32 v95, v95\n\t"
      "v_smfmac_f32_16x16x64_bf16 v[116:119], v[120:123], v[80:87], v156\n\t"
      "s_nop 0\n\t"
      "ds_read_b64_tr_b16 v[146:147], v159 offset:9472\n\t"
      "s_cmp_lt_u32 s64, 6\n\t"
      "s_cbranch_scc1 3f\n\t"
      "s_waitcnt vmcnt(0)\n\t"
      "3:\n\t"
      "s_waitcnt vmcnt(4)\n\t"
      "s_mov_b64 exec, 1\n\t"
      "ds_add_u32 v162, v163\n\t"
      "s_mov_b64 exec, -1\n\t"
      "v_exp_f32 v64, v64\n\t"
      "v_exp_f32 v65, v65\n\t"
      "v_smfmac_f32_32x32x32_bf16 v[16:31], v[152:155], v[88:95], v156\n\t"
      "v_exp_f32 v66, v66\n\t"
      "v_exp_f32 v67, v67\n\t"
      "v_exp_f32 v68, v68\n\t"
      "v_smfmac_f32_16x16x64_bf16 v[116:119], v[120:123], v[88:95], v156\n\t"
      "ds_read_b128 v[132:135], v157 offset:10240\n\t"
      "v_exp_f32 v69, v69\n\t"
      "v_exp_f32 v70, v70\n\t"
      "v_mfma_f32_32x32x16_bf16 v[80:95], v[124:127], v[104:107], v[48:63]\n\t"
      "v_exp_f32 v71, v71\n\t"
      "v_exp_f32 v72, v72\n\t"
      "v_exp_f32 v73, v73\n\t"
      "v_mfma_f32_32x32x16_bf16 v[80:95], v[128:131], v[108:111], v[80:95]\n\t"
      "s_waitcnt lgkmcnt(3)\n\t"
      "v_exp_f32 v74, v74\n\t"
      "v_exp_f32 v75, v75\n\t"
      "v_exp_f32 v76, v76\n\t"
      "v_smfmac_f32_32x32x32_bf16 v[0:15], v[140:143], v[64:71], v156\n\t"
      "s_nop 0\n\t"
      "ds_read_b128 v[136:139], v158 offset:10240\n\t"
      "v_exp_f32 v77, v77\n\t"
      "v_exp_f32 v78, v78\n\t"
      "v_exp_f32 v79, v79\n\t"
      "v_smfmac_f32_16x16x64_bf16 v[112:115], v[120:123], v[64:71], v156\n\t"
      "s_waitcnt lgkmcnt(2)\n\t"
      "ds_read_b64_tr_b16 v[148:149], v159 offset:10240\n\t"
      "v_exp_f32 v80, v80\n\t"
      "v_exp_f32 v81, v81\n\t"
      "v_smfmac_f32_32x32x32_bf16 v[0:15], v[144:147], v[72:79], v156\n\t"
      "v_exp_f32 v82, v82\n\t"
      "v_exp_f32 v83, v83\n\t"
      "v_exp_f32 v84, v84\n\t"
      "v_smfmac_f32_16x16x64_bf16 v[112:115], v[120:123], v[72:79], v156\n\t"
      "s_waitcnt lgkmcnt(2)\n\t"
      "ds_read_b64_tr_b16 v[150:151], v159 offset:10496\n\t"
      "v_exp_f32 v85, v85\n\t"
      "v_exp_f32 v86, v86\n\t"
      "v_mfma_f32_32x32x16_bf16 v[64:79], v[132:135], v[96:99], v[32:47]\n\t"
      "s_waitcnt lgkmcnt(2)\n\t"
      "v_exp_f32 v87, v87\n\t"
      "v_exp_f32 v88, v88\n\t"
      "v_exp_f32 v89, v89\n\t"
      "v_mfma_f32_32x32x16_bf16 v[64:79], v[136:139], v[100:103], v[64:79]\n\t"
      "s_nop 0\n\t"
      "v_exp_f32 v90, v90\n\t"
      "v_exp_f32 v91, v91\n\t"
      "v_exp_f32 v92, v92\n\t"
      "v_smfmac_f32_32x32x32_bf16 v[16:31], v[140:143], v[80:87], v156\n\t"
      "s_nop 0\n\t"
      "ds_read_b64_tr_b16 v[152:153], v159 offset:11264\n\t"
      "v_exp_f32 v93, v93\n\t"
      "v_exp_f32 v94, v94\n\t"
      "v_exp_f32 v95, v95\n\t"
      "v_smfmac_f32_16x16x64_bf16 v[116:119], v[120:123], v[80:87], v156\n\t"
      "s_nop 0\n\t"
      "ds_read_b64_tr_b16 v[154:155], v159 offset:11520\n\t"
      "v_exp_f32 v64, v64\n\t"
      "v_exp_f32 v65, v65\n\t"
      "v_smfmac_f32_32x32x32_bf16 v[16:31], v[144:147], v[88:95], v156\n\t"
      "v_exp_f32 v66, v66\n\t"
      "v_exp_f32 v67, v67\n\t"
      "v_exp_f32 v68, v68\n\t"
      "v_smfmac_f32_16x16x64_bf16 v[116:119], v[120:123], v[88:95], v156\n\t"
      "ds_read_b128 v[124:127], v157 offset:12288\n\t"
      "v_exp_f32 v69, v69\n\t"
      "v_exp_f32 v70, v70\n\t"
      "v_mfma_f32_32x32x16_bf16 v[80:95], v[132:135], v[104:107], v[48:63]\n\t"
      "v_exp_f32 v71, v71\n\t"
      "v_exp_f32 v72, v72\n\t"
      "v_exp_f32 v73, v73\n\t"
      "v_mfma_f32_32x32x16_bf16 v[80:95], v[136:139], v[108:111], v[80:95]\n\t"
      "s_waitcnt lgkmcnt(3)\n\t"
      "v_exp_f32 v74, v74\n\t"
      "v_exp_f32 v75, v75\n\t"
      "v_exp_f32 v76, v76\n\t"
      "v_smfmac_f32_32x32x32_bf16 v[0:15], v[148:151], v[64:71], v156\n\t"
      "s_nop 0\n\t"
      "ds_read_b128 v[128:131], v158 offset:12288\n\t"
      "v_exp_f32 v77, v77\n\t"
      "v_exp_f32 v78, v78\n\t"
      "v_exp_f32 v79, v79\n\t"
      "v_smfmac_f32_16x16x64_bf16 v[112:115], v[120:123], v[64:71], v156\n\t"
      "s_waitcnt lgkmcnt(2)\n\t"
      "ds_read_b64_tr_b16 v[140:141], v159 offset:12288\n\t"
      "v_exp_f32 v80, v80\n\t"
      "v_exp_f32 v81, v81\n\t"
      "v_smfmac_f32_32x32x32_bf16 v[0:15], v[152:155], v[72:79], v156\n\t"
      "v_exp_f32 v82, v82\n\t"
      "v_exp_f32 v83, v83\n\t"
      "v_exp_f32 v84, v84\n\t"
      "v_smfmac_f32_16x16x64_bf16 v[112:115], v[120:123], v[72:79], v156\n\t"
      "s_waitcnt lgkmcnt(2)\n\t"
      "ds_read_b64_tr_b16 v[142:143], v159 offset:12544\n\t"
      "v_exp_f32 v85, v85\n\t"
      "v_exp_f32 v86, v86\n\t"
      "v_mfma_f32_32x32x16_bf16 v[64:79], v[124:127], v[96:99], v[32:47]\n\t"
      "s_waitcnt lgkmcnt(2)\n\t"
      "v_exp_f32 v87, v87\n\t"
      "v_exp_f32 v88, v88\n\t"
      "v_exp_f32 v89, v89\n\t"
      "v_mfma_f32_32x32x16_bf16 v[64:79], v[128:131], v[100:103], v[64:79]\n\t"
      "s_nop 0\n\t"
      "v_exp_f32 v90, v90\n\t"
      "v_exp_f32 v91, v91\n\t"
      "v_exp_f32 v92, v92\n\t"
      "v_smfmac_f32_32x32x32_bf16 v[16:31], v[148:151], v[80:87], v156\n\t"
      "s_nop 0\n\t"
      "ds_read_b64_tr_b16 v[144:145], v159 offset:13312\n\t"
      "v_exp_f32 v93, v93\n\t"
      "v_exp_f32 v94, v94\n\t"
      "v_exp_f32 v95, v95\n\t"
      "v_smfmac_f32_16x16x64_bf16 v[116:119], v[120:123], v[80:87], v156\n\t"
      "s_nop 0\n\t"
      "ds_read_b64_tr_b16 v[146:147], v159 offset:13568\n\t"
      "v_exp_f32 v64, v64\n\t"
      "v_exp_f32 v65, v65\n\t"
      "v_smfmac_f32_32x32x32_bf16 v[16:31], v[152:155], v[88:95], v156\n\t"
      "v_exp_f32 v66, v66\n\t"
      "v_exp_f32 v67, v67\n\t"
      "v_exp_f32 v68, v68\n\t"
      "v_smfmac_f32_16x16x64_bf16 v[116:119], v[120:123], v[88:95], v156\n\t"
      "ds_read_b128 v[132:135], v157 offset:14336\n\t"
      "v_exp_f32 v69, v69\n\t"
      "v_exp_f32 v70, v70\n\t"
      "v_mfma_f32_32x32x16_bf16 v[80:95], v[124:127], v[104:107], v[48:63]\n\t"
      "v_exp_f32 v71, v71\n\t"
      "v_exp_f32 v72, v72\n\t"
      "v_exp_f32 v73, v73\n\t"
      "v_mfma_f32_32x32x16_bf16 v[80:95], v[128:131], v[108:111], v[80:95]\n\t"
      "s_waitcnt lgkmcnt(3)\n\t"
      "v_exp_f32 v74, v74\n\t"
      "v_exp_f32 v75, v75\n\t"
      "v_exp_f32 v76, v76\n\t"
      "v_smfmac_f32_32x32x32_bf16 v[0:15], v[140:143], v[64:71], v156\n\t"
      "s_nop 0\n\t"
      "ds_read_b128 v[136:139], v158 offset:14336\n\t"
      "v_exp_f32 v77, v77\n\t"
      "v_exp_f32 v78, v78\n\t"
      "v_exp_f32 v79, v79\n\t"
      "v_smfmac_f32_16x16x64_bf16 v[112:115], v[120:123], v[64:71], v156\n\t"
      "s_waitcnt lgkmcnt(2)\n\t"
      "ds_read_b64_tr_b16 v[148:149], v159 offset:14336\n\t"
      "v_exp_f32 v80, v80\n\t"
      "v_exp_f32 v81, v81\n\t"
      "v_smfmac_f32_32x32x32_bf16 v[0:15], v[144:147], v[72:79], v156\n\t"
      "ds_read_b32 v164, v162\n\t"
      "v_exp_f32 v82, v82\n\t"
      "v_exp_f32 v83, v83\n\t"
      "v_exp_f32 v84, v84\n\t"
      "v_smfmac_f32_16x16x64_bf16 v[112:115], v[120:123], v[72:79], v156\n\t"
      "s_waitcnt lgkmcnt(3)\n\t"
      "ds_read_b64_tr_b16 v[150:151], v159 offset:14592\n\t"
      "v_exp_f32 v85, v85\n\t"
      "v_exp_f32 v86, v86\n\t"
      "v_mfma_f32_32x32x16_bf16 v[64:79], v[132:135], v[96:99], v[32:47]\n\t"
      "s_waitcnt lgkmcnt(3)\n\t"
      "v_exp_f32 v87, v87\n\t"
      "v_exp_f32 v88, v88\n\t"
      "v_exp_f32 v89, v89\n\t"
      "v_mfma_f32_32x32x16_bf16 v[64:79], v[136:139], v[100:103], v[64:79]\n\t"
      "s_waitcnt lgkmcnt(1)\n\t"
      "v_exp_f32 v90, v90\n\t"
      "v_exp_f32 v91, v91\n\t"
      "v_exp_f32 v92, v92\n\t"
      "v_smfmac_f32_32x32x32_bf16 v[16:31], v[140:143], v[80:87], v156\n\t"
      "s_nop 0\n\t"
      "ds_read_b64_tr_b16 v[152:153], v159 offset:15360\n\t"
      "v_exp_f32 v93, v93\n\t"
      "v_exp_f32 v94, v94\n\t"
      "v_exp_f32 v95, v95\n\t"
      "v_smfmac_f32_16x16x64_bf16 v[116:119], v[120:123], v[80:87], v156\n\t"
      "s_nop 0\n\t"
      "ds_read_b64_tr_b16 v[154:155], v159 offset:15616\n\t"
      "s_cmp_eq_u32 s64, 7\n\t"
      "s_cbranch_scc1 6f\n\t"
      "v_readfirstlane_b32 s68, v164\n\t"
      "s_cmp_ge_u32 s68, s67\n\t"
      "s_cbranch_scc1 5f\n\t"
      "4:\n\t"
      "s_sleep 1\n\t"
      "ds_read_b32 v164, v162\n\t"
      "s_waitcnt lgkmcnt(0)\n\t"
      "v_readfirstlane_b32 s68, v164\n\t"
      "s_cmp_lt_u32 s68, s67\n\t"
      "s_cbranch_scc1 4b\n\t"
      "5:\n\t"
      "s_cmp_gt_u32 s64, 4\n\t"
      "s_cbranch_scc1 6f\n\t"
      "s_add_u32 s66, s64, 3\n\t"
      "s_and_b32 s66, s66, 3\n\t"
      "s_lshl_b32 s66, s66, 15\n\t"
      "s_add_u32 s66, s66, %[dst0]\n\t"
      "s_mov_b32 m0, s66\n\t"
      "s_nop 0\n\t"
      "global_load_lds_dwordx4 v160, s[60:61]\n\t"
      "global_load_lds_dwordx4 v160, s[60:61] offset:1024\n\t"
      "s_add_u32 m0, s66, 0x4000\n\t"
      "s_nop 0\n\t"
      "global_load_lds_dwordx4 v161, s[62:63]\n\t"
      "global_load_lds_dwordx4 v161, s[62:63] offset:1024\n\t"
      "s_add_u32 s60, s60, 0x4000\n\t"
      "s_addc_u32 s61, s61, 0\n\t"
      "s_add_u32 s62, s62, 0x4000\n\t"
      "s_addc_u32 s63, s63, 0\n\t"
      "6:\n\t"
      "s_add_u32 s64, s64, 1\n\t"
      "s_and_b32 s65, s64, 3\n\t"
      "s_lshl_b32 s65, s65, 15\n\t"
      "s_add_u32 s67, s67, 8\n\t"
      "v_add_u32 v157, s65, v165\n\t"
      "v_add_u32 v158, s65, v166\n\t"
      "v_add_u32 v159, s65, v167\n\t"
      "v_exp_f32 v64, v64\n\t"
      "v_exp_f32 v65, v65\n\t"
      "v_smfmac_f32_32x32x32_bf16 v[16:31], v[144:147], v[88:95], v156\n\t"
      "v_exp_f32 v66, v66\n\t"
      "v_exp_f32 v67, v67\n\t"
      "v_exp_f32 v68, v68\n\t"
      "v_smfmac_f32_16x16x64_bf16 v[116:119], v[120:123], v[88:95], v156\n\t"
      "ds_read_b128 v[124:127], v157 offset:0\n\t"
      "v_exp_f32 v69, v69\n\t"
      "v_exp_f32 v70, v70\n\t"
      "v_mfma_f32_32x32x16_bf16 v[80:95], v[132:135], v[104:107], v[48:63]\n\t"
      "v_exp_f32 v71, v71\n\t"
      "v_exp_f32 v72, v72\n\t"
      "v_exp_f32 v73, v73\n\t"
      "v_mfma_f32_32x32x16_bf16 v[80:95], v[136:139], v[108:111], v[80:95]\n\t"
      "s_waitcnt lgkmcnt(3)\n\t"
      "v_exp_f32 v74, v74\n\t"
      "v_exp_f32 v75, v75\n\t"
      "v_exp_f32 v76, v76\n\t"
      "v_smfmac_f32_32x32x32_bf16 v[0:15], v[148:151], v[64:71], v156\n\t"
      "s_nop 0\n\t"
      "ds_read_b128 v[128:131], v158 offset:0\n\t"
      "v_exp_f32 v77, v77\n\t"
      "v_exp_f32 v78, v78\n\t"
      "v_exp_f32 v79, v79\n\t"
      "v_smfmac_f32_16x16x64_bf16 v[112:115], v[120:123], v[64:71], v156\n\t"
      "s_waitcnt lgkmcnt(2)\n\t"
      "ds_read_b64_tr_b16 v[140:141], v159 offset:0\n\t"
      "v_exp_f32 v80, v80\n\t"
      "v_exp_f32 v81, v81\n\t"
      "v_smfmac_f32_32x32x32_bf16 v[0:15], v[152:155], v[72:79], v156\n\t"
      "v_exp_f32 v82, v82\n\t"
      "v_exp_f32 v83, v83\n\t"
      "v_exp_f32 v84, v84\n\t"
      "v_smfmac_f32_16x16x64_bf16 v[112:115], v[120:123], v[72:79], v156\n\t"
      "s_waitcnt lgkmcnt(2)\n\t"
      "ds_read_b64_tr_b16 v[142:143], v159 offset:256\n\t"
      "v_exp_f32 v85, v85\n\t"
      "v_exp_f32 v86, v86\n\t"
      "v_mfma_f32_32x32x16_bf16 v[64:79], v[124:127], v[96:99], v[32:47]\n\t"
      "s_waitcnt lgkmcnt(2)\n\t"
      "v_exp_f32 v87, v87\n\t"
      "v_exp_f32 v88, v88\n\t"
      "v_exp_f32 v89, v89\n\t"
      "v_mfma_f32_32x32x16_bf16 v[64:79], v[128:131], v[100:103], v[64:79]\n\t"
      "s_nop 0\n\t"
      "v_exp_f32 v90, v90\n\t"
      "v_exp_f32 v91, v91\n\t"
      "v_exp_f32 v92, v92\n\t"
      "v_smfmac_f32_32x32x32_bf16 v[16:31], v[148:151], v[80:87], v156\n\t"
      "s_nop 0\n\t"
      "ds_read_b64_tr_b16 v[144:145], v159 offset:1024\n\t"
      "v_exp_f32 v93, v93\n\t"
      "v_exp_f32 v94, v94\n\t"
      "v_exp_f32 v95, v95\n\t"
      "v_smfmac_f32_16x16x64_bf16 v[116:119], v[120:123], v[80:87], v156\n\t"
      "s_cmp_lt_u32 s64, 8\n\t"
      "ds_read_b64_tr_b16 v[146:147], v159 offset:1280\n\t"
      "s_cbranch_scc1 1b\n\t"
      "v_smfmac_f32_32x32x32_bf16 v[16:31], v[152:155], v[88:95], v156\n\t"
      "v_smfmac_f32_16x16x64_bf16 v[116:119], v[120:123], v[88:95], v156\n\t"
      "s_waitcnt lgkmcnt(0)\n\t"
      "s_nop 7\n\t"
      "s_nop 7\n\t"
      : "+{v[0:15]}"(oA), "+{v[16:31]}"(oB), "+{v[64:79]}"(sA), "+{v[80:95]}"(sB), "+{v[112:115]}"(lA), "+{v[116:119]}"(lB), "+{v[124:127]}"(k0), "+{v[128:131]}"(k1), "+{v[140:143]}"(vl), "+{v[144:147]}"(vh), "+{v[152:155]}"(vhz),
        "+{v157}"(kp0u), "+{v158}"(kp1u), "+{v159}"(vpu)
      : "{v[32:47]}"(nmA), "{v[48:63]}"(nmB), "{v[96:99]}"(qA0), "{v[100:103]}"(qA1), "{v[104:107]}"(qB0), "{v[108:111]}"(qB1), "{v[120:123]}"(ones), "{v156}"(sidx),
        "{v160}"(koff), "{v161}"(voff), "{v162}"(cnta), "{v163}"(1u), "{v165}"(kp0u), "{v166}"(kp1u), "{v167}"(vpu),
        [kptr] "s"((const char*)Kh + 2 * CHUNK * 64), [vptr] "s"((const char*)Vh + 2 * CHUNK * 64), [dst0] "s"(dst0)
      : "v132", "v133", "v134", "v135", "v136", "v137", "v138", "v139", "v148", "v149", "v150", "v151", "v164", "s60", "s61", "s62", "s63", "s64", "s65", "s66", "s67", "s68", "m0", "scc", "memory");
  STAMP(3);
  STAMP(4);
  lA[0] = xsum32(lA[0]); lB[0] = xsum32(lB[0]);
  float chk = 0.f;
#pragma unroll
  for (int r = 0; r < 16; ++r) chk += fabsf(oA[r]) + fabsf(oB[r]);
  const bool ok = (lA[0] <= 1.8446744e19f) && (lB[0] <= 1.8446744e19f) && (chk <= 3.0e38f) && (lA[0] >= 0.25f) && (lB[0] >= 0.25f);
  unsigned short* Ow = O + ((size_t)bh * SEQ + q0 + r32) * DH;
  if (__builtin_expect(__all(ok), 1)) {
    store_tile(oA, __builtin_amdgcn_rcpf(lA[0]), Ow, hi);
    store_tile(oB, __builtin_amdgcn_rcpf(lB[0]), Ow + 32 * DH, hi);
  } else {
#pragma unroll 1
    for (int u = 0; u < 2; ++u) slow_tile(Kh, Vh, u ? qB0 : qA0, u ? qB1 : qA1, Ow + u * 32 * DH, lane);
  }
}

extern "C" void kernel_launch(void* const* d_in, const int* in_sizes, int n_in, void* d_out, int out_size, void* d_ws, size_t ws_size, hipStream_t stream) {
  (void)in_sizes; (void)n_in; (void)out_size; (void)d_ws; (void)ws_size;
  (void)hipFuncSetAttribute((const void*)attn_d32_kernel, hipFuncAttributeMaxDynamicSharedMemorySize, LDS_BYTES);
  hipLaunchKernelGGL(attn_d32_kernel, dim3(NBH * (SEQ / QWG)), dim3(NW * 64), LDS_BYTES, stream,
                     (const unsigned short*)d_in[0], (const unsigned short*)d_in[1], (const unsigned short*)d_in[2], (unsigned short*)d_out, (unsigned long long*)d_ws);
}
```

```cpp
#include <hip/hip_runtime.h>
#include <hip/hip_bf16.h>
#include <stdint.h>

typedef __bf16 bf16x8 __attribute__((ext_vector_type(8)));
typedef __bf16 bf16x16 __attribute__((ext_vector_type(16)));
typedef __bf16 bf16x2 __attribute__((ext_vector_type(2)));
typedef float f32x16 __attribute__((ext_vector_type(16)));
typedef float f32x8 __attribute__((ext_vector_type(8)));
typedef float f32x4 __attribute__((ext_vector_type(4)));
typedef float f32x2 __attribute__((ext_vector_type(2)));
typedef unsigned u32x4 __attribute__((ext_vector_type(4)));
typedef short s16x8 __attribute__((ext_vector_type(8)));
typedef __attribute__((address_space(3))) const char* lds_cptr;

constexpr int NBH = 64, SEQ = 2048, DH = 32;
constexpr int NW = 4;
constexpr int QWG = 512;
constexpr int SLOTB = 32768;
constexpr int LDS_BYTES = 4 * SLOTB;
constexpr float QSCALE = 0.17677669529663687f * 1.4426950408889634f;

#define MFMA32(a, b, c) __builtin_amdgcn_mfma_f32_32x32x16_bf16(a, b, c, 0, 0, 0)
#define SM32(a, b, c) __builtin_amdgcn_smfmac_f32_32x32x32_bf16(a, b, c, (int)0xddddddddu, 0, 0)
#define DI __device__ __forceinline__

DI unsigned cvtpk(float lo, float hi) { f32x2 v = {lo, hi}; bf16x2 b = __builtin_convertvector(v, bf16x2); return __builtin_bit_cast(unsigned, b); }
DI bf16x16 lo8(const f32x16& p) { f32x8 t = __builtin_shufflevector(p, p, 0, 1, 2, 3, 4, 5, 6, 7); return __builtin_bit_cast(bf16x16, t); }
DI bf16x16 hi8(const f32x16& p) { f32x8 t = __builtin_shufflevector(p, p, 8, 9, 10, 11, 12, 13, 14, 15); return __builtin_bit_cast(bf16x16, t); }
DI float xmax32(float m) { auto rr = __builtin_amdgcn_permlane32_swap(__float_as_uint(m), __float_as_uint(m), false, false); return fmaxf(__uint_as_float(rr[0]), __uint_as_float(rr[1])); }
DI float xsum32(float m) { auto rr = __builtin_amdgcn_permlane32_swap(__float_as_uint(m), __float_as_uint(m), false, false); return __uint_as_float(rr[0]) + __uint_as_float(rr[1]); }
DI float max16(const f32x16& s) {
  float a = fmaxf(fmaxf(s[0], s[1]), s[2]), b = fmaxf(fmaxf(s[3], s[4]), s[5]);
  a = fmaxf(fmaxf(a, s[6]), s[7]); b = fmaxf(fmaxf(b, s[8]), s[9]);
  a = fmaxf(fmaxf(a, s[10]), s[11]); b = fmaxf(fmaxf(b, s[12]), s[13]);
  a = fmaxf(fmaxf(a, s[14]), s[15]);
  return fmaxf(a, b);
}
DI bf16x8 scaleq(u32x4 w) {
  u32x4 r;
#pragma unroll
  for (int i = 0; i < 4; ++i) r[i] = cvtpk(__uint_as_float(w[i] << 16) * QSCALE, __uint_as_float(w[i] & 0xffff0000u) * QSCALE);
  return __builtin_bit_cast(bf16x8, r);
}
DI void store_tile(const f32x16& o, unsigned short* orow, int hi) {
  unsigned w[8];
#pragma unroll
  for (int i = 0; i < 8; ++i) w[i] = cvtpk(o[2 * i], o[2 * i + 1]);
#pragma unroll
  for (int g = 0; g < 4; g += 2) {
    auto r0 = __builtin_amdgcn_permlane32_swap(w[2 * g], w[2 * g + 2], false, false);
    auto r1 = __builtin_amdgcn_permlane32_swap(w[2 * g + 1], w[2 * g + 3], false, false);
    u32x4 v = {r0[0], r1[0], r0[1], r1[1]};
    __builtin_nontemporal_store(v, (u32x4*)(orow + 8 * g + 8 * hi));
  }
}

DI void slow_tile(const unsigned short* Qrow, const unsigned short* Kh, const unsigned short* Vh, unsigned short* orow, int lane) {
  const int r32 = lane & 31, hi = lane >> 5;
  const bf16x8 q0 = scaleq(*(const u32x4*)(Qrow + hi * 8)), q1 = scaleq(*(const u32x4*)(Qrow + 16 + hi * 8));
  float m = -INFINITY, l = 0.f; f32x16 o = {};
  for (int t = 0; t < SEQ / 32; ++t) {
    const unsigned short* kp = Kh + (t * 32 + r32) * DH + hi * 8;
    bf16x8 k0 = *(const bf16x8*)kp, k1 = *(const bf16x8*)(kp + 16);
    f32x16 s = {};
    s = MFMA32(k0, q0, s); s = MFMA32(k1, q1, s);
    const float mn = fmaxf(m, xmax32(max16(s)));
    const float mu = (mn == -INFINITY) ? 0.f : mn;
    const float alpha = __builtin_amdgcn_exp2f(m - mu);
    m = mn;
    float ps = 0.f;
#pragma unroll
    for (int r = 0; r < 16; ++r) { float p = __builtin_amdgcn_exp2f((s[r] - mu) - 12.f); p = __uint_as_float(__float_as_uint(p) & 0xffff0000u); s[r] = p; ps += p; o[r] *= alpha; }
    l = l * alpha + ps;
    s16x8 v0, v1;
#pragma unroll
    for (int j = 0; j < 8; ++j) { v0[j] = (short)Vh[(t * 32 + hi * 8 + j) * DH + r32]; v1[j] = (short)Vh[(t * 32 + 16 + hi * 8 + j) * DH + r32]; }
    o = SM32(__builtin_bit_cast(bf16x8, v0), lo8(s), o);
    o = SM32(__builtin_bit_cast(bf16x8, v1), hi8(s), o);
  }
  const float rl = 1.0f / xsum32(l);
  store_tile(o * rl, orow, hi);
}

__global__ __launch_bounds__(NW * 64, 1) void attn_d32_kernel(const unsigned short* Q, const unsigned short* K,
                                                                const unsigned short* V, unsigned short* O
                                                                ) {
  extern __shared__ __attribute__((aligned(16))) char lds[];
  const int tid = threadIdx.x, lane = tid & 63, r32 = lane & 31, hi = lane >> 5;
  const int wid = __builtin_amdgcn_readfirstlane(tid >> 6);
  const int bid = blockIdx.x, bh = (bid & 7) * 8 + (bid >> 5), qb = (bid >> 3) & 3;
  const unsigned lds0 = (unsigned)(uintptr_t)(lds_cptr)lds;
  const unsigned kin = (lane >> 2) * 64 + (((lane & 3) ^ ((lane >> 4) & 3)) * 16), vin = lane * 16;
  unsigned t0 = kin + wid * 1024, t1 = vin + wid * 1024, qoff = r32 * 64 + hi * 16;
  unsigned kvoff = bh * (SEQ * DH * 2), rowoff = kvoff + (qb * QWG + wid * 128) * (DH * 2), dst1 = lds0 + wid * 1024;
  asm volatile("" : "+{v235}"(t0), "+{v236}"(t1), "+{v233}"(qoff), "+s"(kvoff), "+s"(rowoff), "+s"(dst1));
  asm volatile("" : "+s"(Q), "+s"(K), "+s"(V), "+s"(O));
  const unsigned short* Kh = (const unsigned short*)((const char*)K + kvoff);
  const unsigned short* Vh = (const unsigned short*)((const char*)V + kvoff);
  const unsigned short* Qw = (const unsigned short*)((const char*)Q + rowoff);
  u32x4 ra0, ra1, rb0, rb1;
  asm volatile(
      "s_mov_b32 m0, %[dst1]\n\t"
      "s_nop 0\n\t"
      "global_load_lds_dwordx4 v235, %[kptr]\n\t"
      "s_add_u32 m0, %[dst1], 16384\n\t"
      "s_nop 0\n\t"
      "global_load_lds_dwordx4 v236, %[vptr]\n\t"
      "global_load_dwordx4 v[64:67], v233, %[qptr] offset:0\n\t"
      "global_load_dwordx4 v[68:71], v233, %[qptr] offset:32\n\t"
      "global_load_dwordx4 v[80:83], v233, %[qptr] offset:2048\n\t"
      "global_load_dwordx4 v[84:87], v233, %[qptr] offset:2080\n\t"
      "s_add_u32 m0, %[dst1], 4096\n\t"
      "v_add_u32 v235, 0x1000, v235\n\t"
      "global_load_lds_dwordx4 v235, %[kptr]\n\t"
      "s_add_u32 m0, %[dst1], 20480\n\t"
      "v_add_u32 v236, 0x1000, v236\n\t"
      "global_load_lds_dwordx4 v236, %[vptr]\n\t"
      : "={v[64:67]}"(ra0), "={v[68:71]}"(ra1), "={v[80:83]}"(rb0), "={v[84:87]}"(rb1), "+{v235}"(t0), "+{v236}"(t1)
      : "{v233}"(qoff), [qptr] "s"(Qw), [kptr] "s"(Kh), [vptr] "s"(Vh), [dst1] "s"(dst1)
      : "m0", "scc", "memory");
  unsigned short* Ow = (unsigned short*)((char*)O + rowoff);
  const int ksw = (r32 >> 2) & 3;
  unsigned kp0 = lds0 + r32 * 64 + ((hi ^ ksw) * 16), kp1 = kp0 ^ 32;
  unsigned vp = lds0 + SLOTB / 2 + (8 * hi + ((lane & 15) >> 2)) * 64 + ((lane >> 4) & 1) * 32 + (lane & 3) * 8;
  const unsigned koff = kin + wid * 4096, voff = vin + wid * 4096, ooff = vin, dst0 = lds0 + wid * 4096;
  const unsigned ew = lds0 + SLOTB + wid * 10240 + r32 * 80 + hi * 8, er = lds0 + SLOTB + wid * 10240 + (lane >> 2) * 80 + (lane & 3) * 16;
  const unsigned kb0 = kp0, kb1 = kp1, vb = vp, sidx = 0xddddddddu;
  const unsigned o1 = 0x3f803f80u, oa = (lane & 15) == 0 ? o1 : 0u, ob = (lane & 15) == 4 ? o1 : 0u;
  u32x4 ones = {oa, oa, ob, ob};
  float l0, l1, l2, l3;
  asm volatile(
      "s_add_u32 s60, %[kpl], 0x4000\n\t"
      "s_addc_u32 s61, %[kph], 0\n\t"
      "s_add_u32 s62, %[vpl], 0x4000\n\t"
      "s_addc_u32 s63, %[vph], 0\n\t"
      "s_mov_b32 s68, 0x3e8293ee\n\t"
      "s_mov_b32 s69, 0x3e8293ee\n\t"
      "v_add_u32 v234, 0x1000, v233\n\t"
      "s_mov_b32 s64, 0\n\t"
      "v_accvgpr_write_b32 a32, v160\n\t"
      "v_accvgpr_write_b32 a33, v161\n\t"
      "v_accvgpr_write_b32 a34, v162\n\t"
      "v_accvgpr_write_b32 a35, v163\n\t"
      "v_mov_b64 v[144:145], 0\n\t"
      "v_mov_b64 v[146:147], 0\n\t"
      "v_mov_b64 v[208:209], 0\n\t"
      "v_mov_b64 v[210:211], 0\n\t"
      "v_mfma_f32_32x32x16_bf16 v[0:15], v[144:147], v[144:147], 0\n\t"
      "v_mov_b64 v[212:213], 0\n\t"
      "v_mov_b64 v[214:215], 0\n\t"
      "v_mfma_f32_32x32x16_bf16 v[16:31], v[144:147], v[144:147], 0\n\t"
      "v_mov_b64 v[216:217], 0\n\t"
      "v_mov_b64 v[218:219], 0\n\t"
      "v_mfma_f32_32x32x16_bf16 v[32:47], v[144:147], v[144:147], 0\n\t"
      "v_mov_b64 v[220:221], 0\n\t"
      "v_mov_b64 v[222:223], 0\n\t"
      "v_mfma_f32_32x32x16_bf16 v[48:63], v[144:147], v[144:147], 0\n\t"
      "s_waitcnt vmcnt(6)\n\t"
      "s_barrier\n\t"
      "global_load_dwordx4 v[96:99], v234, %[qptr] offset:0\n\t"
      "global_load_dwordx4 v[100:103], v234, %[qptr] offset:32\n\t"
      "ds_read_b128 a[0:3], v225 offset:0\n\t"
      "ds_read_b128 a[4:7], v226 offset:0\n\t"
      "ds_read_b128 a[8:11], v225 offset:2048\n\t"
      "ds_read_b128 a[12:15], v226 offset:2048\n\t"
      "ds_read_b64_tr_b16 a[16:17], v227 offset:0\n\t"
      "ds_read_b64_tr_b16 a[18:19], v227 offset:256\n\t"
      "ds_read_b64_tr_b16 a[24:25], v227 offset:1024\n\t"
      "ds_read_b64_tr_b16 a[26:27], v227 offset:1280\n\t"
      "s_add_u32 m0, %[dst1], 8192\n\t"
      "v_add_u32 v235, 0x1000, v235\n\t"
      "global_load_lds_dwordx4 v235, %[kptr]\n\t"
      "s_add_u32 m0, %[dst1], 24576\n\t"
      "v_add_u32 v236, 0x1000, v236\n\t"
      "global_load_lds_dwordx4 v236, %[vptr]\n\t"
      "s_add_u32 m0, %[dst1], 12288\n\t"
      "v_add_u32 v235, 0x1000, v235\n\t"
      "global_load_lds_dwordx4 v235, %[kptr]\n\t"
      "s_add_u32 m0, %[dst1], 28672\n\t"
      "v_add_u32 v236, 0x1000, v236\n\t"
      "global_load_lds_dwordx4 v236, %[vptr]\n\t"
      "s_waitcnt vmcnt(10)\n\t"
      "v_lshlrev_b32 v160, 16, v64\n\t"
      "v_and_b32 v161, 0xffff0000, v64\n\t"
      "v_lshlrev_b32 v162, 16, v65\n\t"
      "v_and_b32 v163, 0xffff0000, v65\n\t"
      "v_lshlrev_b32 v164, 16, v66\n\t"
      "v_and_b32 v165, 0xffff0000, v66\n\t"
      "v_lshlrev_b32 v166, 16, v67\n\t"
      "v_and_b32 v167, 0xffff0000, v67\n\t"
      "v_lshlrev_b32 v168, 16, v68\n\t"
      "v_and_b32 v169, 0xffff0000, v68\n\t"
      "v_lshlrev_b32 v170, 16, v69\n\t"
      "v_and_b32 v171, 0xffff0000, v69\n\t"
      "v_lshlrev_b32 v172, 16, v70\n\t"
      "v_and_b32 v173, 0xffff0000, v70\n\t"
      "v_lshlrev_b32 v174, 16, v71\n\t"
      "v_and_b32 v175, 0xffff0000, v71\n\t"
      "v_pk_mul_f32 v[160:161], v[160:161], s[68:69]\n\t"
      "v_pk_mul_f32 v[162:163], v[162:163], s[68:69]\n\t"
      "v_pk_mul_f32 v[164:165], v[164:165], s[68:69]\n\t"
      "v_pk_mul_f32 v[166:167], v[166:167], s[68:69]\n\t"
      "v_pk_mul_f32 v[168:169], v[168:169], s[68:69]\n\t"
      "v_pk_mul_f32 v[170:171], v[170:171], s[68:69]\n\t"
      "v_pk_mul_f32 v[172:173], v[172:173], s[68:69]\n\t"
      "v_pk_mul_f32 v[174:175], v[174:175], s[68:69]\n\t"
      "v_cvt_pk_bf16_f32 v176, v160, v161\n\t"
      "v_cvt_pk_bf16_f32 v177, v162, v163\n\t"
      "v_cvt_pk_bf16_f32 v178, v164, v165\n\t"
      "v_cvt_pk_bf16_f32 v179, v166, v167\n\t"
      "v_cvt_pk_bf16_f32 v180, v168, v169\n\t"
      "v_cvt_pk_bf16_f32 v181, v170, v171\n\t"
      "v_cvt_pk_bf16_f32 v182, v172, v173\n\t"
      "v_cvt_pk_bf16_f32 v183, v174, v175\n\t"
      "s_waitcnt lgkmcnt(6)\n\t"
      "s_nop 0\n\t"
      "v_mfma_f32_32x32x16_bf16 v[128:143], a[0:3], v[176:179], 0\n\t"
      "v_mfma_f32_32x32x16_bf16 v[128:143], a[4:7], v[180:183], v[128:143]\n\t"
      "s_waitcnt vmcnt(8)\n\t"
      "v_lshlrev_b32 v160, 16, v80\n\t"
      "v_and_b32 v161, 0xffff0000, v80\n\t"
      "v_lshlrev_b32 v162, 16, v81\n\t"
      "v_and_b32 v163, 0xffff0000, v81\n\t"
      "v_lshlrev_b32 v164, 16, v82\n\t"
      "v_and_b32 v165, 0xffff0000, v82\n\t"
      "v_lshlrev_b32 v166, 16, v83\n\t"
      "v_and_b32 v167, 0xffff0000, v83\n\t"
      "v_lshlrev_b32 v168, 16, v84\n\t"
      "v_and_b32 v169, 0xffff0000, v84\n\t"
      "v_lshlrev_b32 v170, 16, v85\n\t"
      "v_and_b32 v171, 0xffff0000, v85\n\t"
      "v_lshlrev_b32 v172, 16, v86\n\t"
      "v_and_b32 v173, 0xffff0000, v86\n\t"
      "v_lshlrev_b32 v174, 16, v87\n\t"
      "v_and_b32 v175, 0xffff0000, v87\n\t"
      "v_pk_mul_f32 v[160:161], v[160:161], s[68:69]\n\t"
      "v_pk_mul_f32 v[162:163], v[162:163], s[68:69]\n\t"
      "v_pk_mul_f32 v[164:165], v[164:165], s[68:69]\n\t"
      "v_pk_mul_f32 v[166:167], v[166:167], s[68:69]\n\t"
      "v_pk_mul_f32 v[168:169], v[168:169], s[68:69]\n\t"
      "v_pk_mul_f32 v[170:171], v[170:171], s[68:69]\n\t"
      "v_pk_mul_f32 v[172:173], v[172:173], s[68:69]\n\t"
      "v_pk_mul_f32 v[174:175], v[174:175], s[68:69]\n\t"
      "v_cvt_pk_bf16_f32 v184, v160, v161\n\t"
      "v_cvt_pk_bf16_f32 v185, v162, v163\n\t"
      "v_cvt_pk_bf16_f32 v186, v164, v165\n\t"
      "v_cvt_pk_bf16_f32 v187, v166, v167\n\t"
      "v_cvt_pk_bf16_f32 v188, v168, v169\n\t"
      "v_cvt_pk_bf16_f32 v189, v170, v171\n\t"
      "v_cvt_pk_bf16_f32 v190, v172, v173\n\t"
      "v_cvt_pk_bf16_f32 v191, v174, v175\n\t"
      "v_max3_f32 v235, v128, v129, v130\n\t"
      "v_max3_f32 v236, v131, v132, v133\n\t"
      "s_waitcnt lgkmcnt(4)\n\t"
      "v_mfma_f32_32x32x16_bf16 v[144:159], a[8:11], v[184:187], 0\n\t"
      "v_max3_f32 v235, v235, v134, v135\n\t"
      "v_max3_f32 v236, v236, v136, v137\n\t"
      "v_max3_f32 v235, v235, v138, v139\n\t"
      "v_max3_f32 v236, v236, v140, v141\n\t"
      "v_max3_f32 v235, v235, v142, v143\n\t"
      "v_max_f32 v235, v235, v236\n\t"
      "v_mfma_f32_32x32x16_bf16 v[144:159], a[12:15], v[188:191], v[144:159]\n\t"
      "v_mov_b32 v236, v235\n\t"
      "s_nop 1\n\t"
      "v_permlane32_swap_b32 v235, v236\n\t"
      "v_max_f32 v235, v235, v236\n\t"
      "v_mul_f32 v64, -1.0, v235\n\t"
      "v_mul_f32 v65, -1.0, v235\n\t"
      "v_mov_b64 v[66:67], v[64:65]\n\t"
      "v_mov_b64 v[68:69], v[64:65]\n\t"
      "v_mov_b64 v[70:71], v[64:65]\n\t"
      "v_mov_b64 v[72:73], v[64:65]\n\t"
      "v_mov_b64 v[74:75], v[64:65]\n\t"
      "v_mov_b64 v[76:77], v[64:65]\n\t"
      "v_mov_b64 v[78:79], v[64:65]\n\t"
      "s_nop 1\n\t"
      "v_mfma_f32_32x32x16_bf16 v[128:143], a[0:3], v[176:179], v[64:79]\n\t"
      "v_mfma_f32_32x32x16_bf16 v[128:143], a[4:7], v[180:183], v[128:143]\n\t"
      "v_max3_f32 v235, v144, v145, v146\n\t"
      "v_max3_f32 v236, v147, v148, v149\n\t"
      "v_max3_f32 v235, v235, v150, v151\n\t"
      "v_max3_f32 v236, v236, v152, v153\n\t"
      "v_max3_f32 v235, v235, v154, v155\n\t"
      "v_max3_f32 v236, v236, v156, v157\n\t"
      "v_max3_f32 v235, v235, v158, v159\n\t"
      "v_max_f32 v235, v235, v236\n\t"
      "v_mov_b32 v236, v235\n\t"
      "s_nop 1\n\t"
      "v_permlane32_swap_b32 v235, v236\n\t"
      "v_max_f32 v235, v235, v236\n\t"
      "v_mul_f32 v80, -1.0, v235\n\t"
      "v_mul_f32 v81, -1.0, v235\n\t"
      "v_mov_b64 v[82:83], v[80:81]\n\t"
      "v_mov_b64 v[84:85], v[80:81]\n\t"
      "v_mov_b64 v[86:87], v[80:81]\n\t"
      "v_mov_b64 v[88:89], v[80:81]\n\t"
      "v_mov_b64 v[90:91], v[80:81]\n\t"
      "v_mov_b64 v[92:93], v[80:81]\n\t"
      "v_mov_b64 v[94:95], v[80:81]\n\t"
      "global_load_dwordx4 v[112:115], v234, %[qptr] offset:2048\n\t"
      "global_load_dwordx4 v[116:119], v234, %[qptr] offset:2080\n\t"
      "v_exp_f32 v128, v128\n\t"
      "v_exp_f32 v129, v129\n\t"
      "v_exp_f32 v130, v130\n\t"
      "v_exp_f32 v131, v131\n\t"
      "v_exp_f32 v132, v132\n\t"
      "s_waitcnt lgkmcnt(4)\n\t"
      "ds_read_b64_tr_b16 a[20:21], v227 offset:2048\n\t"
      "v_exp_f32 v133, v133\n\t"
      "v_exp_f32 v134, v134\n\t"
      "v_mfma_f32_32x32x16_bf16 v[144:159], a[8:11], v[176:179], v[64:79]\n\t"
      "ds_read_b64_tr_b16 a[22:23], v227 offset:2304\n\t"
      "v_exp_f32 v135, v135\n\t"
      "v_exp_f32 v136, v136\n\t"
      "v_exp_f32 v137, v137\n\t"
      "v_mfma_f32_32x32x16_bf16 v[144:159], a[12:15], v[180:183], v[144:159]\n\t"
      "s_waitcnt lgkmcnt(4)\n\t"
      "v_exp_f32 v138, v138\n\t"
      "v_exp_f32 v139, v139\n\t"
      "v_exp_f32 v140, v140\n\t"
      "v_smfmac_f32_32x32x32_bf16 v[0:15], a[16:19], v[128:135], v224\n\t"
      "s_nop 0\n\t"
      "ds_read_b64_tr_b16 a[28:29], v227 offset:3072\n\t"
      "v_exp_f32 v141, v141\n\t"
      "v_exp_f32 v142, v142\n\t"
      "v_exp_f32 v143, v143\n\t"
      "v_smfmac_f32_16x16x64_bf16 v[208:211], a[32:35], v[128:135], v224\n\t"
      "s_waitcnt lgkmcnt(3)\n\t"
      "ds_read_b64_tr_b16 a[30:31], v227 offset:3328\n\t"
      "s_waitcnt vmcnt(8)\n\t"
      "s_barrier\n\t"
      "v_exp_f32 v144, v144\n\t"
      "v_exp_f32 v145, v145\n\t"
      "v_smfmac_f32_32x32x32_bf16 v[0:15], a[24:27], v[136:143], v224\n\t"
      "ds_read_b128 a[0:3], v225 offset:4096\n\t"
      "v_exp_f32 v146, v146\n\t"
      "v_exp_f32 v147, v147\n\t"
      "v_exp_f32 v148, v148\n\t"
      "v_smfmac_f32_16x16x64_bf16 v[208:211], a[32:35], v[136:143], v224\n\t"
      "ds_read_b128 a[4:7], v226 offset:4096\n\t"
      "v_exp_f32 v149, v149\n\t"
      "v_exp_f32 v150, v150\n\t"
      "v_mfma_f32_32x32x16_bf16 v[128:143], a[8:11], v[184:187], v[80:95]\n\t"
      "v_exp_f32 v151, v151\n\t"
      "v_exp_f32 v152, v152\n\t"
      "v_exp_f32 v153, v153\n\t"
      "v_mfma_f32_32x32x16_bf16 v[128:143], a[12:15], v[188:191], v[128:143]\n\t"
      "s_waitcnt lgkmcnt(4)\n\t"
      "v_exp_f32 v154, v154\n\t"
      "v_exp_f32 v155, v155\n\t"
      "v_exp_f32 v156, v156\n\t"
      "v_smfmac_f32_32x32x32_bf16 v[0:15], a[20:23], v[144:151], v224\n\t"
      "s_nop 1\n\t"
      "v_exp_f32 v157, v157\n\t"
      "v_exp_f32 v158, v158\n\t"
      "v_exp_f32 v159, v159\n\t"
      "v_smfmac_f32_16x16x64_bf16 v[208:211], a[32:35], v[144:151], v224\n\t"
      "s_waitcnt lgkmcnt(2)\n\t"
      "ds_read_b64_tr_b16 a[16:17], v227 offset:4096\n\t"
      "v_exp_f32 v128, v128\n\t"
      "v_exp_f32 v129, v129\n\t"
      "v_smfmac_f32_32x32x32_bf16 v[0:15], a[28:31], v[152:159], v224\n\t"
      "v_exp_f32 v130, v130\n\t"
      "v_exp_f32 v131, v131\n\t"
      "v_exp_f32 v132, v132\n\t"
      "v_smfmac_f32_16x16x64_bf16 v[208:211], a[32:35], v[152:159], v224\n\t"
      "s_waitcnt lgkmcnt(1)\n\t"
      "ds_read_b64_tr_b16 a[18:19], v227 offset:4352\n\t"
      "v_exp_f32 v133, v133\n\t"
      "v_exp_f32 v134, v134\n\t"
      "v_mfma_f32_32x32x16_bf16 v[144:159], a[0:3], v[176:179], v[64:79]\n\t"
      "v_exp_f32 v135, v135\n\t"
      "v_exp_f32 v136, v136\n\t"
      "v_exp_f32 v137, v137\n\t"
      "v_mfma_f32_32x32x16_bf16 v[144:159], a[4:7], v[180:183], v[144:159]\n\t"
      "s_nop 0\n\t"
      "v_exp_f32 v138, v138\n\t"
      "v_exp_f32 v139, v139\n\t"
      "v_exp_f32 v140, v140\n\t"
      "v_smfmac_f32_32x32x32_bf16 v[16:31], a[20:23], v[128:135], v224\n\t"
      "s_nop 0\n\t"
      "ds_read_b64_tr_b16 a[24:25], v227 offset:5120\n\t"
      "v_exp_f32 v141, v141\n\t"
      "v_exp_f32 v142, v142\n\t"
      "v_exp_f32 v143, v143\n\t"
      "v_smfmac_f32_16x16x64_bf16 v[212:215], a[32:35], v[128:135], v224\n\t"
      "ds_read_b64_tr_b16 a[26:27], v227 offset:5376\n\t"
      "s_waitcnt vmcnt(6)\n\t"
      "v_lshlrev_b32 v160, 16, v96\n\t"
      "v_and_b32 v161, 0xffff0000, v96\n\t"
      "v_lshlrev_b32 v162, 16, v97\n\t"
      "v_and_b32 v163, 0xffff0000, v97\n\t"
      "v_lshlrev_b32 v164, 16, v98\n\t"
      "v_and_b32 v165, 0xffff0000, v98\n\t"
      "v_lshlrev_b32 v166, 16, v99\n\t"
      "v_and_b32 v167, 0xffff0000, v99\n\t"
      "v_lshlrev_b32 v168, 16, v100\n\t"
      "v_and_b32 v169, 0xffff0000, v100\n\t"
      "v_lshlrev_b32 v170, 16, v101\n\t"
      "v_and_b32 v171, 0xffff0000, v101\n\t"
      "v_lshlrev_b32 v172, 16, v102\n\t"
      "v_and_b32 v173, 0xffff0000, v102\n\t"
      "v_lshlrev_b32 v174, 16, v103\n\t"
      "v_and_b32 v175, 0xffff0000, v103\n\t"
      "v_pk_mul_f32 v[160:161], v[160:161], s[68:69]\n\t"
      "v_pk_mul_f32 v[162:163], v[162:163], s[68:69]\n\t"
      "v_pk_mul_f32 v[164:165], v[164:165], s[68:69]\n\t"
      "v_pk_mul_f32 v[166:167], v[166:167], s[68:69]\n\t"
      "v_pk_mul_f32 v[168:169], v[168:169], s[68:69]\n\t"
      "v_pk_mul_f32 v[170:171], v[170:171], s[68:69]\n\t"
      "v_pk_mul_f32 v[172:173], v[172:173], s[68:69]\n\t"
      "v_pk_mul_f32 v[174:175], v[174:175], s[68:69]\n\t"
      "v_cvt_pk_bf16_f32 v192, v160, v161\n\t"
      "v_cvt_pk_bf16_f32 v193, v162, v163\n\t"
      "v_cvt_pk_bf16_f32 v194, v164, v165\n\t"
      "v_cvt_pk_bf16_f32 v195, v166, v167\n\t"
      "v_cvt_pk_bf16_f32 v196, v168, v169\n\t"
      "v_cvt_pk_bf16_f32 v197, v170, v171\n\t"
      "v_cvt_pk_bf16_f32 v198, v172, v173\n\t"
      "v_cvt_pk_bf16_f32 v199, v174, v175\n\t"
      "s_nop 0\n\t"
      "v_exp_f32 v144, v144\n\t"
      "v_exp_f32 v145, v145\n\t"
      "v_smfmac_f32_32x32x32_bf16 v[16:31], a[28:31], v[136:143], v224\n\t"
      "v_mfma_f32_32x32x16_bf16 v[160:175], a[0:3], v[192:195], 0\n\t"
      "v_exp_f32 v146, v146\n\t"
      "v_exp_f32 v147, v147\n\t"
      "v_exp_f32 v148, v148\n\t"
      "v_smfmac_f32_16x16x64_bf16 v[212:215], a[32:35], v[136:143], v224\n\t"
      "ds_read_b128 a[8:11], v225 offset:6144\n\t"
      "v_exp_f32 v149, v149\n\t"
      "v_exp_f32 v150, v150\n\t"
      "v_mfma_f32_32x32x16_bf16 v[128:143], a[0:3], v[184:187], v[80:95]\n\t"
      "v_mfma_f32_32x32x16_bf16 v[160:175], a[4:7], v[196:199], v[160:175]\n\t"
      "v_exp_f32 v151, v151\n\t"
      "v_exp_f32 v152, v152\n\t"
      "v_exp_f32 v153, v153\n\t"
      "v_mfma_f32_32x32x16_bf16 v[128:143], a[4:7], v[188:191], v[128:143]\n\t"
      "s_waitcnt lgkmcnt(3)\n\t"
      "v_exp_f32 v154, v154\n\t"
      "v_exp_f32 v155, v155\n\t"
      "v_exp_f32 v156, v156\n\t"
      "v_smfmac_f32_32x32x32_bf16 v[0:15], a[16:19], v[144:151], v224\n\t"
      "s_nop 1\n\t"
      "v_exp_f32 v157, v157\n\t"
      "v_exp_f32 v158, v158\n\t"
      "v_exp_f32 v159, v159\n\t"
      "v_smfmac_f32_16x16x64_bf16 v[208:211], a[32:35], v[144:151], v224\n\t"
      "s_waitcnt lgkmcnt(1)\n\t"
      "ds_read_b128 a[12:15], v226 offset:6144\n\t"
      "v_max3_f32 v235, v160, v161, v162\n\t"
      "v_max3_f32 v236, v163, v164, v165\n\t"
      "v_max3_f32 v235, v235, v166, v167\n\t"
      "v_max3_f32 v236, v236, v168, v169\n\t"
      "v_max3_f32 v235, v235, v170, v171\n\t"
      "v_max3_f32 v236, v236, v172, v173\n\t"
      "v_max3_f32 v235, v235, v174, v175\n\t"
      "v_max_f32 v235, v235, v236\n\t"
      "v_mov_b32 v236, v235\n\t"
      "s_nop 1\n\t"
      "v_permlane32_swap_b32 v235, v236\n\t"
      "v_max_f32 v235, v235, v236\n\t"
      "v_mul_f32 v96, -1.0, v235\n\t"
      "v_mul_f32 v97, -1.0, v235\n\t"
      "v_mov_b64 v[98:99], v[96:97]\n\t"
      "v_mov_b64 v[100:101], v[96:97]\n\t"
      "v_mov_b64 v[102:103], v[96:97]\n\t"
      "v_mov_b64 v[104:105], v[96:97]\n\t"
      "v_mov_b64 v[106:107], v[96:97]\n\t"
      "v_mov_b64 v[108:109], v[96:97]\n\t"
      "v_mov_b64 v[110:111], v[96:97]\n\t"
      "v_exp_f32 v128, v128\n\t"
      "v_exp_f32 v129, v129\n\t"
      "v_smfmac_f32_32x32x32_bf16 v[0:15], a[24:27], v[152:159], v224\n\t"
      "v_exp_f32 v130, v130\n\t"
      "v_exp_f32 v131, v131\n\t"
      "v_exp_f32 v132, v132\n\t"
      "v_smfmac_f32_16x16x64_bf16 v[208:211], a[32:35], v[152:159], v224\n\t"
      "ds_read_b64_tr_b16 a[20:21], v227 offset:6144\n\t"
      "v_exp_f32 v133, v133\n\t"
      "v_exp_f32 v134, v134\n\t"
      "v_mfma_f32_32x32x16_bf16 v[144:159], a[0:3], v[192:195], v[96:111]\n\t"
      "v_exp_f32 v135, v135\n\t"
      "v_exp_f32 v136, v136\n\t"
      "v_exp_f32 v137, v137\n\t"
      "v_mfma_f32_32x32x16_bf16 v[144:159], a[4:7], v[196:199], v[144:159]\n\t"
      "s_nop 0\n\t"
      "v_exp_f32 v138, v138\n\t"
      "v_exp_f32 v139, v139\n\t"
      "v_exp_f32 v140, v140\n\t"
      "v_smfmac_f32_32x32x32_bf16 v[16:31], a[16:19], v[128:135], v224\n\t"
      "s_nop 1\n\t"
      "v_exp_f32 v141, v141\n\t"
      "v_exp_f32 v142, v142\n\t"
      "v_exp_f32 v143, v143\n\t"
      "v_smfmac_f32_16x16x64_bf16 v[212:215], a[32:35], v[128:135], v224\n\t"
      "ds_read_b64_tr_b16 a[22:23], v227 offset:6400\n\t"
      "v_exp_f32 v144, v144\n\t"
      "v_exp_f32 v145, v145\n\t"
      "v_smfmac_f32_32x32x32_bf16 v[16:31], a[24:27], v[136:143], v224\n\t"
      "s_add_u32 m0, %[dst0], 32768\n\t"
      "v_exp_f32 v146, v146\n\t"
      "v_exp_f32 v147, v147\n\t"
      "v_exp_f32 v148, v148\n\t"
      "v_smfmac_f32_16x16x64_bf16 v[212:215], a[32:35], v[136:143], v224\n\t"
      "s_waitcnt lgkmcnt(2)\n\t"
      "ds_read_b64_tr_b16 a[28:29], v227 offset:7168\n\t"
      "global_load_lds_dwordx4 v228, s[60:61]\n\t"
      "global_load_lds_dwordx4 v228, s[60:61] offset:1024\n\t"
      "v_exp_f32 v149, v149\n\t"
      "v_exp_f32 v150, v150\n\t"
      "v_mfma_f32_32x32x16_bf16 v[128:143], a[8:11], v[176:179], v[64:79]\n\t"
      "v_exp_f32 v151, v151\n\t"
      "v_exp_f32 v152, v152\n\t"
      "v_exp_f32 v153, v153\n\t"
      "v_mfma_f32_32x32x16_bf16 v[128:143], a[12:15], v[180:183], v[128:143]\n\t"
      "s_nop 0\n\t"
      "v_exp_f32 v154, v154\n\t"
      "v_exp_f32 v155, v155\n\t"
      "v_exp_f32 v156, v156\n\t"
      "v_smfmac_f32_32x32x32_bf16 v[32:47], a[16:19], v[144:151], v224\n\t"
      "global_load_lds_dwordx4 v228, s[60:61] offset:2048\n\t"
      "global_load_lds_dwordx4 v228, s[60:61] offset:3072\n\t"
      "v_exp_f32 v157, v157\n\t"
      "v_exp_f32 v158, v158\n\t"
      "v_exp_f32 v159, v159\n\t"
      "v_smfmac_f32_16x16x64_bf16 v[216:219], a[32:35], v[144:151], v224\n\t"
      "s_add_u32 s60, s60, 0x4000\n\t"
      "s_addc_u32 s61, s61, 0\n\t"
      "ds_read_b64_tr_b16 a[30:31], v227 offset:7424\n\t"
      "s_waitcnt vmcnt(6)\n\t"
      "s_barrier\n\t"
      "s_waitcnt vmcnt(4)\n\t"
      "v_lshlrev_b32 v160, 16, v112\n\t"
      "v_and_b32 v161, 0xffff0000, v112\n\t"
      "v_lshlrev_b32 v162, 16, v113\n\t"
      "v_and_b32 v163, 0xffff0000, v113\n\t"
      "v_lshlrev_b32 v164, 16, v114\n\t"
      "v_and_b32 v165, 0xffff0000, v114\n\t"
      "v_lshlrev_b32 v166, 16, v115\n\t"
      "v_and_b32 v167, 0xffff0000, v115\n\t"
      "v_lshlrev_b32 v168, 16, v116\n\t"
      "v_and_b32 v169, 0xffff0000, v116\n\t"
      "v_lshlrev_b32 v170, 16, v117\n\t"
      "v_and_b32 v171, 0xffff0000, v117\n\t"
      "v_lshlrev_b32 v172, 16, v118\n\t"
      "v_and_b32 v173, 0xffff0000, v118\n\t"
      "v_lshlrev_b32 v174, 16, v119\n\t"
      "v_and_b32 v175, 0xffff0000, v119\n\t"
      "v_pk_mul_f32 v[160:161], v[160:161], s[68:69]\n\t"
      "v_pk_mul_f32 v[162:163], v[162:163], s[68:69]\n\t"
      "v_pk_mul_f32 v[164:165], v[164:165], s[68:69]\n\t"
      "v_pk_mul_f32 v[166:167], v[166:167], s[68:69]\n\t"
      "v_pk_mul_f32 v[168:169], v[168:169], s[68:69]\n\t"
      "v_pk_mul_f32 v[170:171], v[170:171], s[68:69]\n\t"
      "v_pk_mul_f32 v[172:173], v[172:173], s[68:69]\n\t"
      "v_pk_mul_f32 v[174:175], v[174:175], s[68:69]\n\t"
      "v_cvt_pk_bf16_f32 v200, v160, v161\n\t"
      "v_cvt_pk_bf16_f32 v201, v162, v163\n\t"
      "v_cvt_pk_bf16_f32 v202, v164, v165\n\t"
      "v_cvt_pk_bf16_f32 v203, v166, v167\n\t"
      "v_cvt_pk_bf16_f32 v204, v168, v169\n\t"
      "v_cvt_pk_bf16_f32 v205, v170, v171\n\t"
      "v_cvt_pk_bf16_f32 v206, v172, v173\n\t"
      "v_cvt_pk_bf16_f32 v207, v174, v175\n\t"
      "s_nop 0\n\t"
      "v_exp_f32 v128, v128\n\t"
      "v_exp_f32 v129, v129\n\t"
      "v_smfmac_f32_32x32x32_bf16 v[32:47], a[24:27], v[152:159], v224\n\t"
      "v_mfma_f32_32x32x16_bf16 v[160:175], a[8:11], v[200:203], 0\n\t"
      "v_exp_f32 v130, v130\n\t"
      "v_exp_f32 v131, v131\n\t"
      "v_exp_f32 v132, v132\n\t"
      "v_smfmac_f32_16x16x64_bf16 v[216:219], a[32:35], v[152:159], v224\n\t"
      "ds_read_b128 a[0:3], v225 offset:8192\n\t"
      "v_exp_f32 v133, v133\n\t"
      "v_exp_f32 v134, v134\n\t"
      "v_mfma_f32_32x32x16_bf16 v[144:159], a[8:11], v[184:187], v[80:95]\n\t"
      "v_mfma_f32_32x32x16_bf16 v[160:175], a[12:15], v[204:207], v[160:175]\n\t"
      "v_exp_f32 v135, v135\n\t"
      "v_exp_f32 v136, v136\n\t"
      "v_exp_f32 v137, v137\n\t"
      "v_mfma_f32_32x32x16_bf16 v[144:159], a[12:15], v[188:191], v[144:159]\n\t"
      "s_waitcnt lgkmcnt(3)\n\t"
      "v_exp_f32 v138, v138\n\t"
      "v_exp_f32 v139, v139\n\t"
      "v_exp_f32 v140, v140\n\t"
      "v_smfmac_f32_32x32x32_bf16 v[0:15], a[20:23], v[128:135], v224\n\t"
      "s_nop 1\n\t"
      "v_exp_f32 v141, v141\n\t"
      "v_exp_f32 v142, v142\n\t"
      "v_exp_f32 v143, v143\n\t"
      "v_smfmac_f32_16x16x64_bf16 v[208:211], a[32:35], v[128:135], v224\n\t"
      "s_waitcnt lgkmcnt(1)\n\t"
      "ds_read_b128 a[4:7], v226 offset:8192\n\t"
      "v_max3_f32 v235, v160, v161, v162\n\t"
      "v_max3_f32 v236, v163, v164, v165\n\t"
      "v_max3_f32 v235, v235, v166, v167\n\t"
      "v_max3_f32 v236, v236, v168, v169\n\t"
      "v_max3_f32 v235, v235, v170, v171\n\t"
      "v_max3_f32 v236, v236, v172, v173\n\t"
      "v_max3_f32 v235, v235, v174, v175\n\t"
      "v_max_f32 v235, v235, v236\n\t"
      "v_mov_b32 v236, v235\n\t"
      "s_nop 1\n\t"
      "v_permlane32_swap_b32 v235, v236\n\t"
      "v_max_f32 v235, v235, v236\n\t"
      "v_mul_f32 v112, -1.0, v235\n\t"
      "v_mul_f32 v113, -1.0, v235\n\t"
      "v_mov_b64 v[114:115], v[112:113]\n\t"
      "v_mov_b64 v[116:117], v[112:113]\n\t"
      "v_mov_b64 v[118:119], v[112:113]\n\t"
      "v_mov_b64 v[120:121], v[112:113]\n\t"
      "v_mov_b64 v[122:123], v[112:113]\n\t"
      "v_mov_b64 v[124:125], v[112:113]\n\t"
      "v_mov_b64 v[126:127], v[112:113]\n\t"
      "v_exp_f32 v144, v144\n\t"
      "v_exp_f32 v145, v145\n\t"
      "v_smfmac_f32_32x32x32_bf16 v[0:15], a[28:31], v[136:143], v224\n\t"
      "v_exp_f32 v146, v146\n\t"
      "v_exp_f32 v147, v147\n\t"
      "v_exp_f32 v148, v148\n\t"
      "v_smfmac_f32_16x16x64_bf16 v[208:211], a[32:35], v[136:143], v224\n\t"
      "ds_read_b64_tr_b16 a[16:17], v227 offset:8192\n\t"
      "v_exp_f32 v149, v149\n\t"
      "v_exp_f32 v150, v150\n\t"
      "v_mfma_f32_32x32x16_bf16 v[128:143], a[8:11], v[192:195], v[96:111]\n\t"
      "v_exp_f32 v151, v151\n\t"
      "v_exp_f32 v152, v152\n\t"
      "v_exp_f32 v153, v153\n\t"
      "v_mfma_f32_32x32x16_bf16 v[128:143], a[12:15], v[196:199], v[128:143]\n\t"
      "s_nop 0\n\t"
      "v_exp_f32 v154, v154\n\t"
      "v_exp_f32 v155, v155\n\t"
      "v_exp_f32 v156, v156\n\t"
      "v_smfmac_f32_32x32x32_bf16 v[16:31], a[20:23], v[144:151], v224\n\t"
      "s_nop 1\n\t"
      "v_exp_f32 v157, v157\n\t"
      "v_exp_f32 v158, v158\n\t"
      "v_exp_f32 v159, v159\n\t"
      "v_smfmac_f32_16x16x64_bf16 v[212:215], a[32:35], v[144:151], v224\n\t"
      "ds_read_b64_tr_b16 a[18:19], v227 offset:8448\n\t"
      "v_exp_f32 v128, v128\n\t"
      "v_exp_f32 v129, v129\n\t"
      "v_smfmac_f32_32x32x32_bf16 v[16:31], a[28:31], v[152:159], v224\n\t"
      "v_exp_f32 v130, v130\n\t"
      "v_exp_f32 v131, v131\n\t"
      "v_exp_f32 v132, v132\n\t"
      "v_smfmac_f32_16x16x64_bf16 v[212:215], a[32:35], v[152:159], v224\n\t"
      "ds_read_b64_tr_b16 a[24:25], v227 offset:9216\n\t"
      "v_exp_f32 v133, v133\n\t"
      "v_exp_f32 v134, v134\n\t"
      "v_mfma_f32_32x32x16_bf16 v[144:159], a[8:11], v[200:203], v[112:127]\n\t"
      "v_exp_f32 v135, v135\n\t"
      "v_exp_f32 v136, v136\n\t"
      "v_exp_f32 v137, v137\n\t"
      "v_mfma_f32_32x32x16_bf16 v[144:159], a[12:15], v[204:207], v[144:159]\n\t"
      "s_nop 0\n\t"
      "v_exp_f32 v138, v138\n\t"
      "v_exp_f32 v139, v139\n\t"
      "v_exp_f32 v140, v140\n\t"
      "v_smfmac_f32_32x32x32_bf16 v[32:47], a[20:23], v[128:135], v224\n\t"
      "s_nop 1\n\t"
      "v_exp_f32 v141, v141\n\t"
      "v_exp_f32 v142, v142\n\t"
      "v_exp_f32 v143, v143\n\t"
      "v_smfmac_f32_16x16x64_bf16 v[216:219], a[32:35], v[128:135], v224\n\t"
      "ds_read_b64_tr_b16 a[26:27], v227 offset:9472\n\t"
      "v_exp_f32 v144, v144\n\t"
      "v_exp_f32 v145, v145\n\t"
      "v_smfmac_f32_32x32x32_bf16 v[32:47], a[28:31], v[136:143], v224\n\t"
      "s_add_u32 m0, %[dst0], 49152\n\t"
      "v_exp_f32 v146, v146\n\t"
      "v_exp_f32 v147, v147\n\t"
      "v_exp_f32 v148, v148\n\t"
      "v_smfmac_f32_16x16x64_bf16 v[216:219], a[32:35], v[136:143], v224\n\t"
      "s_waitcnt lgkmcnt(4)\n\t"
      "global_load_lds_dwordx4 v229, s[62:63]\n\t"
      "global_load_lds_dwordx4 v229, s[62:63] offset:1024\n\t"
      "v_exp_f32 v149, v149\n\t"
      "v_exp_f32 v150, v150\n\t"
      "v_mfma_f32_32x32x16_bf16 v[128:143], a[0:3], v[176:179], v[64:79]\n\t"
      "v_exp_f32 v151, v151\n\t"
      "v_exp_f32 v152, v152\n\t"
      "v_exp_f32 v153, v153\n\t"
      "v_mfma_f32_32x32x16_bf16 v[128:143], a[4:7], v[180:183], v[128:143]\n\t"
      "s_nop 0\n\t"
      "v_exp_f32 v154, v154\n\t"
      "v_exp_f32 v155, v155\n\t"
      "v_exp_f32 v156, v156\n\t"
      "v_smfmac_f32_32x32x32_bf16 v[48:63], a[20:23], v[144:151], v224\n\t"
      "global_load_lds_dwordx4 v229, s[62:63] offset:2048\n\t"
      "global_load_lds_dwordx4 v229, s[62:63] offset:3072\n\t"
      "v_exp_f32 v157, v157\n\t"
      "v_exp_f32 v158, v158\n\t"
      "v_exp_f32 v159, v159\n\t"
      "v_smfmac_f32_16x16x64_bf16 v[220:223], a[32:35], v[144:151], v224\n\t"
      "s_add_u32 s62, s62, 0x4000\n\t"
      "s_addc_u32 s63, s63, 0\n\t"
      "v_exp_f32 v128, v128\n\t"
      "v_exp_f32 v129, v129\n\t"
      "v_smfmac_f32_32x32x32_bf16 v[48:63], a[28:31], v[152:159], v224\n\t"
      "v_exp_f32 v130, v130\n\t"
      "v_exp_f32 v131, v131\n\t"
      "v_exp_f32 v132, v132\n\t"
      "v_smfmac_f32_16x16x64_bf16 v[220:223], a[32:35], v[152:159], v224\n\t"
      "ds_read_b128 a[8:11], v225 offset:10240\n\t"
      "v_exp_f32 v133, v133\n\t"
      "v_exp_f32 v134, v134\n\t"
      "v_mfma_f32_32x32x16_bf16 v[144:159], a[0:3], v[184:187], v[80:95]\n\t"
      "v_exp_f32 v135, v135\n\t"
      "v_exp_f32 v136, v136\n\t"
      "v_exp_f32 v137, v137\n\t"
      "v_mfma_f32_32x32x16_bf16 v[144:159], a[4:7], v[188:191], v[144:159]\n\t"
      "s_waitcnt lgkmcnt(3)\n\t"
      "v_exp_f32 v138, v138\n\t"
      "v_exp_f32 v139, v139\n\t"
      "v_exp_f32 v140, v140\n\t"
      "v_smfmac_f32_32x32x32_bf16 v[0:15], a[16:19], v[128:135], v224\n\t"
      "s_nop 1\n\t"
      "v_exp_f32 v141, v141\n\t"
      "v_exp_f32 v142, v142\n\t"
      "v_exp_f32 v143, v143\n\t"
      "v_smfmac_f32_16x16x64_bf16 v[208:211], a[32:35], v[128:135], v224\n\t"
      "s_waitcnt lgkmcnt(1)\n\t"
      "ds_read_b128 a[12:15], v226 offset:10240\n\t"
      "v_exp_f32 v144, v144\n\t"
      "v_exp_f32 v145, v145\n\t"
      "v_smfmac_f32_32x32x32_bf16 v[0:15], a[24:27], v[136:143], v224\n\t"
      "v_exp_f32 v146, v146\n\t"
      "v_exp_f32 v147, v147\n\t"
      "v_exp_f32 v148, v148\n\t"
      "v_smfmac_f32_16x16x64_bf16 v[208:211], a[32:35], v[136:143], v224\n\t"
      "ds_read_b64_tr_b16 a[20:21], v227 offset:10240\n\t"
      "v_exp_f32 v149, v149\n\t"
      "v_exp_f32 v150, v150\n\t"
      "v_mfma_f32_32x32x16_bf16 v[128:143], a[0:3], v[192:195], v[96:111]\n\t"
      "v_exp_f32 v151, v151\n\t"
      "v_exp_f32 v152, v152\n\t"
      "v_exp_f32 v153, v153\n\t"
      "v_mfma_f32_32x32x16_bf16 v[128:143], a[4:7], v[196:199], v[128:143]\n\t"
      "s_nop 0\n\t"
      "v_exp_f32 v154, v154\n\t"
      "v_exp_f32 v155, v155\n\t"
      "v_exp_f32 v156, v156\n\t"
      "v_smfmac_f32_32x32x32_bf16 v[16:31], a[16:19], v[144:151], v224\n\t"
      "s_nop 1\n\t"
      "v_exp_f32 v157, v157\n\t"
      "v_exp_f32 v158, v158\n\t"
      "v_exp_f32 v159, v159\n\t"
      "v_smfmac_f32_16x16x64_bf16 v[212:215], a[32:35], v[144:151], v224\n\t"
      "ds_read_b64_tr_b16 a[22:23], v227 offset:10496\n\t"
      "v_exp_f32 v128, v128\n\t"
      "v_exp_f32 v129, v129\n\t"
      "v_smfmac_f32_32x32x32_bf16 v[16:31], a[24:27], v[152:159], v224\n\t"
      "v_exp_f32 v130, v130\n\t"
      "v_exp_f32 v131, v131\n\t"
      "v_exp_f32 v132, v132\n\t"
      "v_smfmac_f32_16x16x64_bf16 v[212:215], a[32:35], v[152:159], v224\n\t"
      "ds_read_b64_tr_b16 a[28:29], v227 offset:11264\n\t"
      "v_exp_f32 v133, v133\n\t"
      "v_exp_f32 v134, v134\n\t"
      "v_mfma_f32_32x32x16_bf16 v[144:159], a[0:3], v[200:203], v[112:127]\n\t"
      "v_exp_f32 v135, v135\n\t"
      "v_exp_f32 v136, v136\n\t"
      "v_exp_f32 v137, v137\n\t"
      "v_mfma_f32_32x32x16_bf16 v[144:159], a[4:7], v[204:207], v[144:159]\n\t"
      "s_nop 0\n\t"
      "v_exp_f32 v138, v138\n\t"
      "v_exp_f32 v139, v139\n\t"
      "v_exp_f32 v140, v140\n\t"
      "v_smfmac_f32_32x32x32_bf16 v[32:47], a[16:19], v[128:135], v224\n\t"
      "s_nop 1\n\t"
      "v_exp_f32 v141, v141\n\t"
      "v_exp_f32 v142, v142\n\t"
      "v_exp_f32 v143, v143\n\t"
      "v_smfmac_f32_16x16x64_bf16 v[216:219], a[32:35], v[128:135], v224\n\t"
      "ds_read_b64_tr_b16 a[30:31], v227 offset:11520\n\t"
      "v_exp_f32 v144, v144\n\t"
      "v_exp_f32 v145, v145\n\t"
      "v_smfmac_f32_32x32x32_bf16 v[32:47], a[24:27], v[136:143], v224\n\t"
      "s_add_u32 m0, %[dst0], 65536\n\t"
      "v_exp_f32 v146, v146\n\t"
      "v_exp_f32 v147, v147\n\t"
      "v_exp_f32 v148, v148\n\t"
      "v_smfmac_f32_16x16x64_bf16 v[216:219], a[32:35], v[136:143], v224\n\t"
      "s_waitcnt lgkmcnt(4)\n\t"
      "global_load_lds_dwordx4 v228, s[60:61]\n\t"
      "global_load_lds_dwordx4 v228, s[60:61] offset:1024\n\t"
      "v_exp_f32 v149, v149\n\t"
      "v_exp_f32 v150, v150\n\t"
      "v_mfma_f32_32x32x16_bf16 v[128:143], a[8:11], v[176:179], v[64:79]\n\t"
      "v_exp_f32 v151, v151\n\t"
      "v_exp_f32 v152, v152\n\t"
      "v_exp_f32 v153, v153\n\t"
      "v_mfma_f32_32x32x16_bf16 v[128:143], a[12:15], v[180:183], v[128:143]\n\t"
      "s_nop 0\n\t"
      "v_exp_f32 v154, v154\n\t"
      "v_exp_f32 v155, v155\n\t"
      "v_exp_f32 v156, v156\n\t"
      "v_smfmac_f32_32x32x32_bf16 v[48:63], a[16:19], v[144:151], v224\n\t"
      "global_load_lds_dwordx4 v228, s[60:61] offset:2048\n\t"
      "global_load_lds_dwordx4 v228, s[60:61] offset:3072\n\t"
      "v_exp_f32 v157, v157\n\t"
      "v_exp_f32 v158, v158\n\t"
      "v_exp_f32 v159, v159\n\t"
      "v_smfmac_f32_16x16x64_bf16 v[220:223], a[32:35], v[144:151], v224\n\t"
      "s_add_u32 s60, s60, 0x4000\n\t"
      "s_addc_u32 s61, s61, 0\n\t"
      "v_exp_f32 v128, v128\n\t"
      "v_exp_f32 v129, v129\n\t"
      "v_smfmac_f32_32x32x32_bf16 v[48:63], a[24:27], v[152:159], v224\n\t"
      "v_exp_f32 v130, v130\n\t"
      "v_exp_f32 v131, v131\n\t"
      "v_exp_f32 v132, v132\n\t"
      "v_smfmac_f32_16x16x64_bf16 v[220:223], a[32:35], v[152:159], v224\n\t"
      "ds_read_b128 a[0:3], v225 offset:12288\n\t"
      "v_exp_f32 v133, v133\n\t"
      "v_exp_f32 v134, v134\n\t"
      "v_mfma_f32_32x32x16_bf16 v[144:159], a[8:11], v[184:187], v[80:95]\n\t"
      "v_exp_f32 v135, v135\n\t"
      "v_exp_f32 v136, v136\n\t"
      "v_exp_f32 v137, v137\n\t"
      "v_mfma_f32_32x32x16_bf16 v[144:159], a[12:15], v[188:191], v[144:159]\n\t"
      "s_waitcnt lgkmcnt(3)\n\t"
      "v_exp_f32 v138, v138\n\t"
      "v_exp_f32 v139, v139\n\t"
      "v_exp_f32 v140, v140\n\t"
      "v_smfmac_f32_32x32x32_bf16 v[0:15], a[20:23], v[128:135], v224\n\t"
      "s_nop 1\n\t"
      "v_exp_f32 v141, v141\n\t"
      "v_exp_f32 v142, v142\n\t"
      "v_exp_f32 v143, v143\n\t"
      "v_smfmac_f32_16x16x64_bf16 v[208:211], a[32:35], v[128:135], v224\n\t"
      "s_waitcnt lgkmcnt(1)\n\t"
      "ds_read_b128 a[4:7], v226 offset:12288\n\t"
      "v_exp_f32 v144, v144\n\t"
      "v_exp_f32 v145, v145\n\t"
      "v_smfmac_f32_32x32x32_bf16 v[0:15], a[28:31], v[136:143], v224\n\t"
      "v_exp_f32 v146, v146\n\t"
      "v_exp_f32 v147, v147\n\t"
      "v_exp_f32 v148, v148\n\t"
      "v_smfmac_f32_16x16x64_bf16 v[208:211], a[32:35], v[136:143], v224\n\t"
      "ds_read_b64_tr_b16 a[16:17], v227 offset:12288\n\t"
      "v_exp_f32 v149, v149\n\t"
      "v_exp_f32 v150, v150\n\t"
      "v_mfma_f32_32x32x16_bf16 v[128:143], a[8:11], v[192:195], v[96:111]\n\t"
      "v_exp_f32 v151, v151\n\t"
      "v_exp_f32 v152, v152\n\t"
      "v_exp_f32 v153, v153\n\t"
      "v_mfma_f32_32x32x16_bf16 v[128:143], a[12:15], v[196:199], v[128:143]\n\t"
      "s_nop 0\n\t"
      "v_exp_f32 v154, v154\n\t"
      "v_exp_f32 v155, v155\n\t"
      "v_exp_f32 v156, v156\n\t"
      "v_smfmac_f32_32x32x32_bf16 v[16:31], a[20:23], v[144:151], v224\n\t"
      "s_nop 1\n\t"
      "v_exp_f32 v157, v157\n\t"
      "v_exp_f32 v158, v158\n\t"
      "v_exp_f32 v159, v159\n\t"
      "v_smfmac_f32_16x16x64_bf16 v[212:215], a[32:35], v[144:151], v224\n\t"
      "ds_read_b64_tr_b16 a[18:19], v227 offset:12544\n\t"
      "v_exp_f32 v128, v128\n\t"
      "v_exp_f32 v129, v129\n\t"
      "v_smfmac_f32_32x32x32_bf16 v[16:31], a[28:31], v[152:159], v224\n\t"
      "v_exp_f32 v130, v130\n\t"
      "v_exp_f32 v131, v131\n\t"
      "v_exp_f32 v132, v132\n\t"
      "v_smfmac_f32_16x16x64_bf16 v[212:215], a[32:35], v[152:159], v224\n\t"
      "ds_read_b64_tr_b16 a[24:25], v227 offset:13312\n\t"
      "v_exp_f32 v133, v133\n\t"
      "v_exp_f32 v134, v134\n\t"
      "v_mfma_f32_32x32x16_bf16 v[144:159], a[8:11], v[200:203], v[112:127]\n\t"
      "v_exp_f32 v135, v135\n\t"
      "v_exp_f32 v136, v136\n\t"
      "v_exp_f32 v137, v137\n\t"
      "v_mfma_f32_32x32x16_bf16 v[144:159], a[12:15], v[204:207], v[144:159]\n\t"
      "s_nop 0\n\t"
      "v_exp_f32 v138, v138\n\t"
      "v_exp_f32 v139, v139\n\t"
      "v_exp_f32 v140, v140\n\t"
      "v_smfmac_f32_32x32x32_bf16 v[32:47], a[20:23], v[128:135], v224\n\t"
      "s_nop 1\n\t"
      "v_exp_f32 v141, v141\n\t"
      "v_exp_f32 v142, v142\n\t"
      "v_exp_f32 v143, v143\n\t"
      "v_smfmac_f32_16x16x64_bf16 v[216:219], a[32:35], v[128:135], v224\n\t"
      "ds_read_b64_tr_b16 a[26:27], v227 offset:13568\n\t"
      "v_exp_f32 v144, v144\n\t"
      "v_exp_f32 v145, v145\n\t"
      "v_smfmac_f32_32x32x32_bf16 v[32:47], a[28:31], v[136:143], v224\n\t"
      "s_add_u32 m0, %[dst0], 81920\n\t"
      "v_exp_f32 v146, v146\n\t"
      "v_exp_f32 v147, v147\n\t"
      "v_exp_f32 v148, v148\n\t"
      "v_smfmac_f32_16x16x64_bf16 v[216:219], a[32:35], v[136:143], v224\n\t"
      "s_waitcnt lgkmcnt(4)\n\t"
      "global_load_lds_dwordx4 v229, s[62:63]\n\t"
      "global_load_lds_dwordx4 v229, s[62:63] offset:1024\n\t"
      "v_exp_f32 v149, v149\n\t"
      "v_exp_f32 v150, v150\n\t"
      "v_mfma_f32_32x32x16_bf16 v[128:143], a[0:3], v[176:179], v[64:79]\n\t"
      "v_exp_f32 v151, v151\n\t"
      "v_exp_f32 v152, v152\n\t"
      "v_exp_f32 v153, v153\n\t"
      "v_mfma_f32_32x32x16_bf16 v[128:143], a[4:7], v[180:183], v[128:143]\n\t"
      "s_nop 0\n\t"
      "v_exp_f32 v154, v154\n\t"
      "v_exp_f32 v155, v155\n\t"
      "v_exp_f32 v156, v156\n\t"
      "v_smfmac_f32_32x32x32_bf16 v[48:63], a[20:23], v[144:151], v224\n\t"
      "global_load_lds_dwordx4 v229, s[62:63] offset:2048\n\t"
      "global_load_lds_dwordx4 v229, s[62:63] offset:3072\n\t"
      "v_exp_f32 v157, v157\n\t"
      "v_exp_f32 v158, v158\n\t"
      "v_exp_f32 v159, v159\n\t"
      "v_smfmac_f32_16x16x64_bf16 v[220:223], a[32:35], v[144:151], v224\n\t"
      "s_add_u32 s62, s62, 0x4000\n\t"
      "s_addc_u32 s63, s63, 0\n\t"
      "v_exp_f32 v128, v128\n\t"
      "v_exp_f32 v129, v129\n\t"
      "v_smfmac_f32_32x32x32_bf16 v[48:63], a[28:31], v[152:159], v224\n\t"
      "v_exp_f32 v130, v130\n\t"
      "v_exp_f32 v131, v131\n\t"
      "v_exp_f32 v132, v132\n\t"
      "v_smfmac_f32_16x16x64_bf16 v[220:223], a[32:35], v[152:159], v224\n\t"
      "ds_read_b128 a[8:11], v225 offset:14336\n\t"
      "v_exp_f32 v133, v133\n\t"
      "v_exp_f32 v134, v134\n\t"
      "v_mfma_f32_32x32x16_bf16 v[144:159], a[0:3], v[184:187], v[80:95]\n\t"
      "v_exp_f32 v135, v135\n\t"
      "v_exp_f32 v136, v136\n\t"
      "v_exp_f32 v137, v137\n\t"
      "v_mfma_f32_32x32x16_bf16 v[144:159], a[4:7], v[188:191], v[144:159]\n\t"
      "s_waitcnt lgkmcnt(3)\n\t"
      "v_exp_f32 v138, v138\n\t"
      "v_exp_f32 v139, v139\n\t"
      "v_exp_f32 v140, v140\n\t"
      "v_smfmac_f32_32x32x32_bf16 v[0:15], a[16:19], v[128:135], v224\n\t"
      "s_nop 1\n\t"
      "v_exp_f32 v141, v141\n\t"
      "v_exp_f32 v142, v142\n\t"
      "v_exp_f32 v143, v143\n\t"
      "v_smfmac_f32_16x16x64_bf16 v[208:211], a[32:35], v[128:135], v224\n\t"
      "s_waitcnt lgkmcnt(1)\n\t"
      "ds_read_b128 a[12:15], v226 offset:14336\n\t"
      "v_exp_f32 v144, v144\n\t"
      "v_exp_f32 v145, v145\n\t"
      "v_smfmac_f32_32x32x32_bf16 v[0:15], a[24:27], v[136:143], v224\n\t"
      "s_add_u32 s66, %[dst0], 98304\n\t"
      "s_add_u32 s67, %[dst0], 114688\n\t"
      "v_exp_f32 v146, v146\n\t"
      "v_exp_f32 v147, v147\n\t"
      "v_exp_f32 v148, v148\n\t"
      "v_smfmac_f32_16x16x64_bf16 v[208:211], a[32:35], v[136:143], v224\n\t"
      "ds_read_b64_tr_b16 a[20:21], v227 offset:14336\n\t"
      "v_exp_f32 v149, v149\n\t"
      "v_exp_f32 v150, v150\n\t"
      "v_mfma_f32_32x32x16_bf16 v[128:143], a[0:3], v[192:195], v[96:111]\n\t"
      "v_exp_f32 v151, v151\n\t"
      "v_exp_f32 v152, v152\n\t"
      "v_exp_f32 v153, v153\n\t"
      "v_mfma_f32_32x32x16_bf16 v[128:143], a[4:7], v[196:199], v[128:143]\n\t"
      "s_nop 0\n\t"
      "v_exp_f32 v154, v154\n\t"
      "v_exp_f32 v155, v155\n\t"
      "v_exp_f32 v156, v156\n\t"
      "v_smfmac_f32_32x32x32_bf16 v[16:31], a[16:19], v[144:151], v224\n\t"
      "s_nop 1\n\t"
      "v_exp_f32 v157, v157\n\t"
      "v_exp_f32 v158, v158\n\t"
      "v_exp_f32 v159, v159\n\t"
      "v_smfmac_f32_16x16x64_bf16 v[212:215], a[32:35], v[144:151], v224\n\t"
      "ds_read_b64_tr_b16 a[22:23], v227 offset:14592\n\t"
      "v_exp_f32 v128, v128\n\t"
      "v_exp_f32 v129, v129\n\t"
      "v_smfmac_f32_32x32x32_bf16 v[16:31], a[24:27], v[152:159], v224\n\t"
      "s_add_u32 s64, s64, 1\n\t"
      "s_and_b32 s65, s64, 3\n\t"
      "v_exp_f32 v130, v130\n\t"
      "v_exp_f32 v131, v131\n\t"
      "v_exp_f32 v132, v132\n\t"
      "v_smfmac_f32_16x16x64_bf16 v[212:215], a[32:35], v[152:159], v224\n\t"
      "ds_read_b64_tr_b16 a[28:29], v227 offset:15360\n\t"
      "v_exp_f32 v133, v133\n\t"
      "v_exp_f32 v134, v134\n\t"
      "v_mfma_f32_32x32x16_bf16 v[144:159], a[0:3], v[200:203], v[112:127]\n\t"
      "v_exp_f32 v135, v135\n\t"
      "v_exp_f32 v136, v136\n\t"
      "v_exp_f32 v137, v137\n\t"
      "v_mfma_f32_32x32x16_bf16 v[144:159], a[4:7], v[204:207], v[144:159]\n\t"
      "s_nop 0\n\t"
      "v_exp_f32 v138, v138\n\t"
      "v_exp_f32 v139, v139\n\t"
      "v_exp_f32 v140, v140\n\t"
      "v_smfmac_f32_32x32x32_bf16 v[32:47], a[16:19], v[128:135], v224\n\t"
      "s_nop 1\n\t"
      "v_exp_f32 v141, v141\n\t"
      "v_exp_f32 v142, v142\n\t"
      "v_exp_f32 v143, v143\n\t"
      "v_smfmac_f32_16x16x64_bf16 v[216:219], a[32:35], v[128:135], v224\n\t"
      "ds_read_b64_tr_b16 a[30:31], v227 offset:15616\n\t"
      "v_exp_f32 v144, v144\n\t"
      "v_exp_f32 v145, v145\n\t"
      "v_smfmac_f32_32x32x32_bf16 v[32:47], a[24:27], v[136:143], v224\n\t"
      "s_lshl_b32 s65, s65, 15\n\t"
      "v_exp_f32 v146, v146\n\t"
      "v_exp_f32 v147, v147\n\t"
      "v_exp_f32 v148, v148\n\t"
      "v_smfmac_f32_16x16x64_bf16 v[216:219], a[32:35], v[136:143], v224\n\t"
      "s_waitcnt lgkmcnt(4)\n\t"
      "v_exp_f32 v149, v149\n\t"
      "v_exp_f32 v150, v150\n\t"
      "v_mfma_f32_32x32x16_bf16 v[128:143], a[8:11], v[176:179], v[64:79]\n\t"
      "v_exp_f32 v151, v151\n\t"
      "v_exp_f32 v152, v152\n\t"
      "v_exp_f32 v153, v153\n\t"
      "v_mfma_f32_32x32x16_bf16 v[128:143], a[12:15], v[180:183], v[128:143]\n\t"
      "s_nop 0\n\t"
      "v_exp_f32 v154, v154\n\t"
      "v_exp_f32 v155, v155\n\t"
      "v_exp_f32 v156, v156\n\t"
      "v_smfmac_f32_32x32x32_bf16 v[48:63], a[16:19], v[144:151], v224\n\t"
      "s_waitcnt vmcnt(8)\n\t"
      "s_barrier\n\t"
      "v_exp_f32 v157, v157\n\t"
      "v_exp_f32 v158, v158\n\t"
      "v_exp_f32 v159, v159\n\t"
      "v_smfmac_f32_16x16x64_bf16 v[220:223], a[32:35], v[144:151], v224\n\t"
      "s_nop 0\n\t"
      "v_add_u32 v225, s65, v230\n\t"
      "v_add_u32 v226, s65, v231\n\t"
      "v_add_u32 v227, s65, v232\n\t"
      "v_exp_f32 v128, v128\n\t"
      "v_exp_f32 v129, v129\n\t"
      "v_smfmac_f32_32x32x32_bf16 v[48:63], a[24:27], v[152:159], v224\n\t"
      "v_exp_f32 v130, v130\n\t"
      "v_exp_f32 v131, v131\n\t"
      "v_exp_f32 v132, v132\n\t"
      "v_smfmac_f32_16x16x64_bf16 v[220:223], a[32:35], v[152:159], v224\n\t"
      "ds_read_b128 a[0:3], v225 offset:0\n\t"
      "v_exp_f32 v133, v133\n\t"
      "v_exp_f32 v134, v134\n\t"
      "v_mfma_f32_32x32x16_bf16 v[144:159], a[8:11], v[184:187], v[80:95]\n\t"
      "v_exp_f32 v135, v135\n\t"
      "v_exp_f32 v136, v136\n\t"
      "v_exp_f32 v137, v137\n\t"
      "v_mfma_f32_32x32x16_bf16 v[144:159], a[12:15], v[188:191], v[144:159]\n\t"
      "s_waitcnt lgkmcnt(3)\n\t"
      "v_exp_f32 v138, v138\n\t"
      "v_exp_f32 v139, v139\n\t"
      "v_exp_f32 v140, v140\n\t"
      "v_smfmac_f32_32x32x32_bf16 v[0:15], a[20:23], v[128:135], v224\n\t"
      "s_nop 1\n\t"
      "v_exp_f32 v141, v141\n\t"
      "v_exp_f32 v142, v142\n\t"
      "v_exp_f32 v143, v143\n\t"
      "v_smfmac_f32_16x16x64_bf16 v[208:211], a[32:35], v[128:135], v224\n\t"
      "s_waitcnt lgkmcnt(1)\n\t"
      "ds_read_b128 a[4:7], v226 offset:0\n\t"
      "v_exp_f32 v144, v144\n\t"
      "v_exp_f32 v145, v145\n\t"
      "v_smfmac_f32_32x32x32_bf16 v[0:15], a[28:31], v[136:143], v224\n\t"
      "v_exp_f32 v146, v146\n\t"
      "v_exp_f32 v147, v147\n\t"
      "v_exp_f32 v148, v148\n\t"
      "v_smfmac_f32_16x16x64_bf16 v[208:211], a[32:35], v[136:143], v224\n\t"
      "ds_read_b64_tr_b16 a[16:17], v227 offset:0\n\t"
      "v_exp_f32 v149, v149\n\t"
      "v_exp_f32 v150, v150\n\t"
      "v_mfma_f32_32x32x16_bf16 v[128:143], a[8:11], v[192:195], v[96:111]\n\t"
      "v_exp_f32 v151, v151\n\t"
      "v_exp_f32 v152, v152\n\t"
      "v_exp_f32 v153, v153\n\t"
      "v_mfma_f32_32x32x16_bf16 v[128:143], a[12:15], v[196:199], v[128:143]\n\t"
      "s_nop 0\n\t"
      "v_exp_f32 v154, v154\n\t"
      "v_exp_f32 v155, v155\n\t"
      "v_exp_f32 v156, v156\n\t"
      "v_smfmac_f32_32x32x32_bf16 v[16:31], a[20:23], v[144:151], v224\n\t"
      "s_nop 1\n\t"
      "v_exp_f32 v157, v157\n\t"
      "v_exp_f32 v158, v158\n\t"
      "v_exp_f32 v159, v159\n\t"
      "v_smfmac_f32_16x16x64_bf16 v[212:215], a[32:35], v[144:151], v224\n\t"
      "ds_read_b64_tr_b16 a[18:19], v227 offset:256\n\t"
      "v_exp_f32 v128, v128\n\t"
      "v_exp_f32 v129, v129\n\t"
      "v_smfmac_f32_32x32x32_bf16 v[16:31], a[28:31], v[152:159], v224\n\t"
      "v_exp_f32 v130, v130\n\t"
      "v_exp_f32 v131, v131\n\t"
      "v_exp_f32 v132, v132\n\t"
      "v_smfmac_f32_16x16x64_bf16 v[212:215], a[32:35], v[152:159], v224\n\t"
      "ds_read_b64_tr_b16 a[24:25], v227 offset:1024\n\t"
      "v_exp_f32 v133, v133\n\t"
      "v_exp_f32 v134, v134\n\t"
      "v_mfma_f32_32x32x16_bf16 v[144:159], a[8:11], v[200:203], v[112:127]\n\t"
      "v_exp_f32 v135, v135\n\t"
      "v_exp_f32 v136, v136\n\t"
      "v_exp_f32 v137, v137\n\t"
      "v_mfma_f32_32x32x16_bf16 v[144:159], a[12:15], v[204:207], v[144:159]\n\t"
      "s_nop 0\n\t"
      "v_exp_f32 v138, v138\n\t"
      "v_exp_f32 v139, v139\n\t"
      "v_exp_f32 v140, v140\n\t"
      "v_smfmac_f32_32x32x32_bf16 v[32:47], a[20:23], v[128:135], v224\n\t"
      "s_nop 1\n\t"
      "v_exp_f32 v141, v141\n\t"
      "v_exp_f32 v142, v142\n\t"
      "v_exp_f32 v143, v143\n\t"
      "v_smfmac_f32_16x16x64_bf16 v[216:219], a[32:35], v[128:135], v224\n\t"
      "ds_read_b64_tr_b16 a[26:27], v227 offset:1280\n\t"
      "v_exp_f32 v144, v144\n\t"
      "v_exp_f32 v145, v145\n\t"
      "v_smfmac_f32_32x32x32_bf16 v[32:47], a[28:31], v[136:143], v224\n\t"
      "v_exp_f32 v146, v146\n\t"
      "v_exp_f32 v147, v147\n\t"
      "v_exp_f32 v148, v148\n\t"
      "v_smfmac_f32_16x16x64_bf16 v[216:219], a[32:35], v[136:143], v224\n\t"
      "s_waitcnt lgkmcnt(4)\n\t"
      "v_exp_f32 v149, v149\n\t"
      "v_exp_f32 v150, v150\n\t"
      "v_mfma_f32_32x32x16_bf16 v[128:143], a[0:3], v[176:179], v[64:79]\n\t"
      "v_exp_f32 v151, v151\n\t"
      "v_exp_f32 v152, v152\n\t"
      "v_exp_f32 v153, v153\n\t"
      "v_mfma_f32_32x32x16_bf16 v[128:143], a[4:7], v[180:183], v[128:143]\n\t"
      "s_nop 0\n\t"
      "v_exp_f32 v154, v154\n\t"
      "v_exp_f32 v155, v155\n\t"
      "v_exp_f32 v156, v156\n\t"
      "v_smfmac_f32_32x32x32_bf16 v[48:63], a[20:23], v[144:151], v224\n\t"
      "s_nop 1\n\t"
      "v_exp_f32 v157, v157\n\t"
      "v_exp_f32 v158, v158\n\t"
      "v_exp_f32 v159, v159\n\t"
      "v_smfmac_f32_16x16x64_bf16 v[220:223], a[32:35], v[144:151], v224\n\t"
      "s_nop 0\n\t"
      ".p2align 6\n\t"
      "1:\n\t"
      "v_exp_f32 v128, v128\n\t"
      "v_exp_f32 v129, v129\n\t"
      "v_smfmac_f32_32x32x32_bf16 v[48:63], a[28:31], v[152:159], v224\n\t"
      "v_exp_f32 v130, v130\n\t"
      "v_exp_f32 v131, v131\n\t"
      "v_exp_f32 v132, v132\n\t"
      "v_smfmac_f32_16x16x64_bf16 v[220:223], a[32:35], v[152:159], v224\n\t"
      "ds_read_b128 a[8:11], v225 offset:2048\n\t"
      "v_exp_f32 v133, v133\n\t"
      "v_exp_f32 v134, v134\n\t"
      "v_mfma_f32_32x32x16_bf16 v[144:159], a[0:3], v[184:187], v[80:95]\n\t"
      "v_exp_f32 v135, v135\n\t"
      "v_exp_f32 v136, v136\n\t"
      "v_exp_f32 v137, v137\n\t"
      "v_mfma_f32_32x32x16_bf16 v[144:159], a[4:7], v[188:191], v[144:159]\n\t"
      "s_waitcnt lgkmcnt(3)\n\t"
      "v_exp_f32 v138, v138\n\t"
      "v_exp_f32 v139, v139\n\t"
      "v_exp_f32 v140, v140\n\t"
      "v_smfmac_f32_32x32x32_bf16 v[0:15], a[16:19], v[128:135], v224\n\t"
      "s_nop 1\n\t"
      "v_exp_f32 v141, v141\n\t"
      "v_exp_f32 v142, v142\n\t"
      "v_exp_f32 v143, v143\n\t"
      "v_smfmac_f32_16x16x64_bf16 v[208:211], a[32:35], v[128:135], v224\n\t"
      "s_waitcnt lgkmcnt(1)\n\t"
      "ds_read_b128 a[12:15], v226 offset:2048\n\t"
      "v_exp_f32 v144, v144\n\t"
      "v_exp_f32 v145, v145\n\t"
      "v_smfmac_f32_32x32x32_bf16 v[0:15], a[24:27], v[136:143], v224\n\t"
      "v_exp_f32 v146, v146\n\t"
      "v_exp_f32 v147, v147\n\t"
      "v_exp_f32 v148, v148\n\t"
      "v_smfmac_f32_16x16x64_bf16 v[208:211], a[32:35], v[136:143], v224\n\t"
      "ds_read_b64_tr_b16 a[20:21], v227 offset:2048\n\t"
      "v_exp_f32 v149, v149\n\t"
      "v_exp_f32 v150, v150\n\t"
      "v_mfma_f32_32x32x16_bf16 v[128:143], a[0:3], v[192:195], v[96:111]\n\t"
      "v_exp_f32 v151, v151\n\t"
      "v_exp_f32 v152, v152\n\t"
      "v_exp_f32 v153, v153\n\t"
      "v_mfma_f32_32x32x16_bf16 v[128:143], a[4:7], v[196:199], v[128:143]\n\t"
      "s_nop 0\n\t"
      "v_exp_f32 v154, v154\n\t"
      "v_exp_f32 v155, v155\n\t"
      "v_exp_f32 v156, v156\n\t"
      "v_smfmac_f32_32x32x32_bf16 v[16:31], a[16:19], v[144:151], v224\n\t"
      "s_nop 1\n\t"
      "v_exp_f32 v157, v157\n\t"
      "v_exp_f32 v158, v158\n\t"
      "v_exp_f32 v159, v159\n\t"
      "v_smfmac_f32_16x16x64_bf16 v[212:215], a[32:35], v[144:151], v224\n\t"
      "ds_read_b64_tr_b16 a[22:23], v227 offset:2304\n\t"
      "v_exp_f32 v128, v128\n\t"
      "v_exp_f32 v129, v129\n\t"
      "v_smfmac_f32_32x32x32_bf16 v[16:31], a[24:27], v[152:159], v224\n\t"
      "v_exp_f32 v130, v130\n\t"
      "v_exp_f32 v131, v131\n\t"
      "v_exp_f32 v132, v132\n\t"
      "v_smfmac_f32_16x16x64_bf16 v[212:215], a[32:35], v[152:159], v224\n\t"
      "ds_read_b64_tr_b16 a[28:29], v227 offset:3072\n\t"
      "v_exp_f32 v133, v133\n\t"
      "v_exp_f32 v134, v134\n\t"
      "v_mfma_f32_32x32x16_bf16 v[144:159], a[0:3], v[200:203], v[112:127]\n\t"
      "v_exp_f32 v135, v135\n\t"
      "v_exp_f32 v136, v136\n\t"
      "v_exp_f32 v137, v137\n\t"
      "v_mfma_f32_32x32x16_bf16 v[144:159], a[4:7], v[204:207], v[144:159]\n\t"
      "s_nop 0\n\t"
      "v_exp_f32 v138, v138\n\t"
      "v_exp_f32 v139, v139\n\t"
      "v_exp_f32 v140, v140\n\t"
      "v_smfmac_f32_32x32x32_bf16 v[32:47], a[16:19], v[128:135], v224\n\t"
      "s_nop 1\n\t"
      "v_exp_f32 v141, v141\n\t"
      "v_exp_f32 v142, v142\n\t"
      "v_exp_f32 v143, v143\n\t"
      "v_smfmac_f32_16x16x64_bf16 v[216:219], a[32:35], v[128:135], v224\n\t"
      "ds_read_b64_tr_b16 a[30:31], v227 offset:3328\n\t"
      "v_exp_f32 v144, v144\n\t"
      "v_exp_f32 v145, v145\n\t"
      "v_smfmac_f32_32x32x32_bf16 v[32:47], a[24:27], v[136:143], v224\n\t"
      "s_mov_b32 m0, s66\n\t"
      "v_exp_f32 v146, v146\n\t"
      "v_exp_f32 v147, v147\n\t"
      "v_exp_f32 v148, v148\n\t"
      "v_smfmac_f32_16x16x64_bf16 v[216:219], a[32:35], v[136:143], v224\n\t"
      "s_waitcnt lgkmcnt(4)\n\t"
      "global_load_lds_dwordx4 v228, s[60:61]\n\t"
      "v_exp_f32 v149, v149\n\t"
      "v_exp_f32 v150, v150\n\t"
      "v_mfma_f32_32x32x16_bf16 v[128:143], a[8:11], v[176:179], v[64:79]\n\t"
      "v_exp_f32 v151, v151\n\t"
      "v_exp_f32 v152, v152\n\t"
      "v_exp_f32 v153, v153\n\t"
      "v_mfma_f32_32x32x16_bf16 v[128:143], a[12:15], v[180:183], v[128:143]\n\t"
      "s_nop 0\n\t"
      "v_exp_f32 v154, v154\n\t"
      "v_exp_f32 v155, v155\n\t"
      "v_exp_f32 v156, v156\n\t"
      "v_smfmac_f32_32x32x32_bf16 v[48:63], a[16:19], v[144:151], v224\n\t"
      "s_nop 1\n\t"
      "v_exp_f32 v157, v157\n\t"
      "v_exp_f32 v158, v158\n\t"
      "v_exp_f32 v159, v159\n\t"
      "v_smfmac_f32_16x16x64_bf16 v[220:223], a[32:35], v[144:151], v224\n\t"
      "global_load_lds_dwordx4 v228, s[60:61] offset:1024\n\t"
      "v_exp_f32 v128, v128\n\t"
      "v_exp_f32 v129, v129\n\t"
      "v_smfmac_f32_32x32x32_bf16 v[48:63], a[24:27], v[152:159], v224\n\t"
      "v_exp_f32 v130, v130\n\t"
      "v_exp_f32 v131, v131\n\t"
      "v_exp_f32 v132, v132\n\t"
      "v_smfmac_f32_16x16x64_bf16 v[220:223], a[32:35], v[152:159], v224\n\t"
      "ds_read_b128 a[0:3], v225 offset:4096\n\t"
      "v_exp_f32 v133, v133\n\t"
      "v_exp_f32 v134, v134\n\t"
      "v_mfma_f32_32x32x16_bf16 v[144:159], a[8:11], v[184:187], v[80:95]\n\t"
      "v_exp_f32 v135, v135\n\t"
      "v_exp_f32 v136, v136\n\t"
      "v_exp_f32 v137, v137\n\t"
      "v_mfma_f32_32x32x16_bf16 v[144:159], a[12:15], v[188:191], v[144:159]\n\t"
      "s_waitcnt lgkmcnt(3)\n\t"
      "v_exp_f32 v138, v138\n\t"
      "v_exp_f32 v139, v139\n\t"
      "v_exp_f32 v140, v140\n\t"
      "v_smfmac_f32_32x32x32_bf16 v[0:15], a[20:23], v[128:135], v224\n\t"
      "s_nop 1\n\t"
      "v_exp_f32 v141, v141\n\t"
      "v_exp_f32 v142, v142\n\t"
      "v_exp_f32 v143, v143\n\t"
      "v_smfmac_f32_16x16x64_bf16 v[208:211], a[32:35], v[128:135], v224\n\t"
      "s_waitcnt lgkmcnt(1)\n\t"
      "ds_read_b128 a[4:7], v226 offset:4096\n\t"
      "v_exp_f32 v144, v144\n\t"
      "v_exp_f32 v145, v145\n\t"
      "v_smfmac_f32_32x32x32_bf16 v[0:15], a[28:31], v[136:143], v224\n\t"
      "v_exp_f32 v146, v146\n\t"
      "v_exp_f32 v147, v147\n\t"
      "v_exp_f32 v148, v148\n\t"
      "v_smfmac_f32_16x16x64_bf16 v[208:211], a[32:35], v[136:143], v224\n\t"
      "ds_read_b64_tr_b16 a[16:17], v227 offset:4096\n\t"
      "v_exp_f32 v149, v149\n\t"
      "v_exp_f32 v150, v150\n\t"
      "v_mfma_f32_32x32x16_bf16 v[128:143], a[8:11], v[192:195], v[96:111]\n\t"
      "v_exp_f32 v151, v151\n\t"
      "v_exp_f32 v152, v152\n\t"
      "v_exp_f32 v153, v153\n\t"
      "v_mfma_f32_32x32x16_bf16 v[128:143], a[12:15], v[196:199], v[128:143]\n\t"
      "s_nop 0\n\t"
      "v_exp_f32 v154, v154\n\t"
      "v_exp_f32 v155, v155\n\t"
      "v_exp_f32 v156, v156\n\t"
      "v_smfmac_f32_32x32x32_bf16 v[16:31], a[20:23], v[144:151], v224\n\t"
      "s_nop 1\n\t"
      "v_exp_f32 v157, v157\n\t"
      "v_exp_f32 v158, v158\n\t"
      "v_exp_f32 v159, v159\n\t"
      "v_smfmac_f32_16x16x64_bf16 v[212:215], a[32:35], v[144:151], v224\n\t"
      "ds_read_b64_tr_b16 a[18:19], v227 offset:4352\n\t"
      "v_exp_f32 v128, v128\n\t"
      "v_exp_f32 v129, v129\n\t"
      "v_smfmac_f32_32x32x32_bf16 v[16:31], a[28:31], v[152:159], v224\n\t"
      "v_exp_f32 v130, v130\n\t"
      "v_exp_f32 v131, v131\n\t"
      "v_exp_f32 v132, v132\n\t"
      "v_smfmac_f32_16x16x64_bf16 v[212:215], a[32:35], v[152:159], v224\n\t"
      "ds_read_b64_tr_b16 a[24:25], v227 offset:5120\n\t"
      "v_exp_f32 v133, v133\n\t"
      "v_exp_f32 v134, v134\n\t"
      "v_mfma_f32_32x32x16_bf16 v[144:159], a[8:11], v[200:203], v[112:127]\n\t"
      "v_exp_f32 v135, v135\n\t"
      "v_exp_f32 v136, v136\n\t"
      "v_exp_f32 v137, v137\n\t"
      "v_mfma_f32_32x32x16_bf16 v[144:159], a[12:15], v[204:207], v[144:159]\n\t"
      "s_nop 0\n\t"
      "v_exp_f32 v138, v138\n\t"
      "v_exp_f32 v139, v139\n\t"
      "v_exp_f32 v140, v140\n\t"
      "v_smfmac_f32_32x32x32_bf16 v[32:47], a[20:23], v[128:135], v224\n\t"
      "s_nop 1\n\t"
      "v_exp_f32 v141, v141\n\t"
      "v_exp_f32 v142, v142\n\t"
      "v_exp_f32 v143, v143\n\t"
      "v_smfmac_f32_16x16x64_bf16 v[216:219], a[32:35], v[128:135], v224\n\t"
      "ds_read_b64_tr_b16 a[26:27], v227 offset:5376\n\t"
      "v_exp_f32 v144, v144\n\t"
      "v_exp_f32 v145, v145\n\t"
      "v_smfmac_f32_32x32x32_bf16 v[32:47], a[28:31], v[136:143], v224\n\t"
      "v_exp_f32 v146, v146\n\t"
      "v_exp_f32 v147, v147\n\t"
      "v_exp_f32 v148, v148\n\t"
      "v_smfmac_f32_16x16x64_bf16 v[216:219], a[32:35], v[136:143], v224\n\t"
      "s_waitcnt lgkmcnt(4)\n\t"
      "global_load_lds_dwordx4 v228, s[60:61] offset:2048\n\t"
      "v_exp_f32 v149, v149\n\t"
      "v_exp_f32 v150, v150\n\t"
      "v_mfma_f32_32x32x16_bf16 v[128:143], a[0:3], v[176:179], v[64:79]\n\t"
      "v_exp_f32 v151, v151\n\t"
      "v_exp_f32 v152, v152\n\t"
      "v_exp_f32 v153, v153\n\t"
      "v_mfma_f32_32x32x16_bf16 v[128:143], a[4:7], v[180:183], v[128:143]\n\t"
      "s_nop 0\n\t"
      "v_exp_f32 v154, v154\n\t"
      "v_exp_f32 v155, v155\n\t"
      "v_exp_f32 v156, v156\n\t"
      "v_smfmac_f32_32x32x32_bf16 v[48:63], a[20:23], v[144:151], v224\n\t"
      "s_nop 1\n\t"
      "v_exp_f32 v157, v157\n\t"
      "v_exp_f32 v158, v158\n\t"
      "v_exp_f32 v159, v159\n\t"
      "v_smfmac_f32_16x16x64_bf16 v[220:223], a[32:35], v[144:151], v224\n\t"
      "global_load_lds_dwordx4 v228, s[60:61] offset:3072\n\t"
      "v_exp_f32 v128, v128\n\t"
      "v_exp_f32 v129, v129\n\t"
      "v_smfmac_f32_32x32x32_bf16 v[48:63], a[28:31], v[152:159], v224\n\t"
      "s_add_u32 s60, s60, 0x4000\n\t"
      "s_addc_u32 s61, s61, 0\n\t"
      "v_exp_f32 v130, v130\n\t"
      "v_exp_f32 v131, v131\n\t"
      "v_exp_f32 v132, v132\n\t"
      "v_smfmac_f32_16x16x64_bf16 v[220:223], a[32:35], v[152:159], v224\n\t"
      "ds_read_b128 a[8:11], v225 offset:6144\n\t"
      "v_exp_f32 v133, v133\n\t"
      "v_exp_f32 v134, v134\n\t"
      "v_mfma_f32_32x32x16_bf16 v[144:159], a[0:3], v[184:187], v[80:95]\n\t"
      "v_exp_f32 v135, v135\n\t"
      "v_exp_f32 v136, v136\n\t"
      "v_exp_f32 v137, v137\n\t"
      "v_mfma_f32_32x32x16_bf16 v[144:159], a[4:7], v[188:191], v[144:159]\n\t"
      "s_waitcnt lgkmcnt(3)\n\t"
      "v_exp_f32 v138, v138\n\t"
      "v_exp_f32 v139, v139\n\t"
      "v_exp_f32 v140, v140\n\t"
      "v_smfmac_f32_32x32x32_bf16 v[0:15], a[16:19], v[128:135], v224\n\t"
      "s_nop 1\n\t"
      "v_exp_f32 v141, v141\n\t"
      "v_exp_f32 v142, v142\n\t"
      "v_exp_f32 v143, v143\n\t"
      "v_smfmac_f32_16x16x64_bf16 v[208:211], a[32:35], v[128:135], v224\n\t"
      "s_waitcnt lgkmcnt(1)\n\t"
      "ds_read_b128 a[12:15], v226 offset:6144\n\t"
      "v_exp_f32 v144, v144\n\t"
      "v_exp_f32 v145, v145\n\t"
      "v_smfmac_f32_32x32x32_bf16 v[0:15], a[24:27], v[136:143], v224\n\t"
      "v_exp_f32 v146, v146\n\t"
      "v_exp_f32 v147, v147\n\t"
      "v_exp_f32 v148, v148\n\t"
      "v_smfmac_f32_16x16x64_bf16 v[208:211], a[32:35], v[136:143], v224\n\t"
      "ds_read_b64_tr_b16 a[20:21], v227 offset:6144\n\t"
      "v_exp_f32 v149, v149\n\t"
      "v_exp_f32 v150, v150\n\t"
      "v_mfma_f32_32x32x16_bf16 v[128:143], a[0:3], v[192:195], v[96:111]\n\t"
      "v_exp_f32 v151, v151\n\t"
      "v_exp_f32 v152, v152\n\t"
      "v_exp_f32 v153, v153\n\t"
      "v_mfma_f32_32x32x16_bf16 v[128:143], a[4:7], v[196:199], v[128:143]\n\t"
      "s_nop 0\n\t"
      "v_exp_f32 v154, v154\n\t"
      "v_exp_f32 v155, v155\n\t"
      "v_exp_f32 v156, v156\n\t"
      "v_smfmac_f32_32x32x32_bf16 v[16:31], a[16:19], v[144:151], v224\n\t"
      "s_nop 1\n\t"
      "v_exp_f32 v157, v157\n\t"
      "v_exp_f32 v158, v158\n\t"
      "v_exp_f32 v159, v159\n\t"
      "v_smfmac_f32_16x16x64_bf16 v[212:215], a[32:35], v[144:151], v224\n\t"
      "ds_read_b64_tr_b16 a[22:23], v227 offset:6400\n\t"
      "v_exp_f32 v128, v128\n\t"
      "v_exp_f32 v129, v129\n\t"
      "v_smfmac_f32_32x32x32_bf16 v[16:31], a[24:27], v[152:159], v224\n\t"
      "v_exp_f32 v130, v130\n\t"
      "v_exp_f32 v131, v131\n\t"
      "v_exp_f32 v132, v132\n\t"
      "v_smfmac_f32_16x16x64_bf16 v[212:215], a[32:35], v[152:159], v224\n\t"
      "ds_read_b64_tr_b16 a[28:29], v227 offset:7168\n\t"
      "v_exp_f32 v133, v133\n\t"
      "v_exp_f32 v134, v134\n\t"
      "v_mfma_f32_32x32x16_bf16 v[144:159], a[0:3], v[200:203], v[112:127]\n\t"
      "v_exp_f32 v135, v135\n\t"
      "v_exp_f32 v136, v136\n\t"
      "v_exp_f32 v137, v137\n\t"
      "v_mfma_f32_32x32x16_bf16 v[144:159], a[4:7], v[204:207], v[144:159]\n\t"
      "s_nop 0\n\t"
      "v_exp_f32 v138, v138\n\t"
      "v_exp_f32 v139, v139\n\t"
      "v_exp_f32 v140, v140\n\t"
      "v_smfmac_f32_32x32x32_bf16 v[32:47], a[16:19], v[128:135], v224\n\t"
      "s_nop 1\n\t"
      "v_exp_f32 v141, v141\n\t"
      "v_exp_f32 v142, v142\n\t"
      "v_exp_f32 v143, v143\n\t"
      "v_smfmac_f32_16x16x64_bf16 v[216:219], a[32:35], v[128:135], v224\n\t"
      "ds_read_b64_tr_b16 a[30:31], v227 offset:7424\n\t"
      "v_exp_f32 v144, v144\n\t"
      "v_exp_f32 v145, v145\n\t"
      "v_smfmac_f32_32x32x32_bf16 v[32:47], a[24:27], v[136:143], v224\n\t"
      "s_mov_b32 m0, s67\n\t"
      "v_exp_f32 v146, v146\n\t"
      "v_exp_f32 v147, v147\n\t"
      "v_exp_f32 v148, v148\n\t"
      "v_smfmac_f32_16x16x64_bf16 v[216:219], a[32:35], v[136:143], v224\n\t"
      "s_waitcnt lgkmcnt(4)\n\t"
      "global_load_lds_dwordx4 v229, s[62:63]\n\t"
      "v_exp_f32 v149, v149\n\t"
      "v_exp_f32 v150, v150\n\t"
      "v_mfma_f32_32x32x16_bf16 v[128:143], a[8:11], v[176:179], v[64:79]\n\t"
      "v_exp_f32 v151, v151\n\t"
      "v_exp_f32 v152, v152\n\t"
      "v_exp_f32 v153, v153\n\t"
      "v_mfma_f32_32x32x16_bf16 v[128:143], a[12:15], v[180:183], v[128:143]\n\t"
      "s_nop 0\n\t"
      "v_exp_f32 v154, v154\n\t"
      "v_exp_f32 v155, v155\n\t"
      "v_exp_f32 v156, v156\n\t"
      "v_smfmac_f32_32x32x32_bf16 v[48:63], a[16:19], v[144:151], v224\n\t"
      "s_nop 1\n\t"
      "v_exp_f32 v157, v157\n\t"
      "v_exp_f32 v158, v158\n\t"
      "v_exp_f32 v159, v159\n\t"
      "v_smfmac_f32_16x16x64_bf16 v[220:223], a[32:35], v[144:151], v224\n\t"
      "global_load_lds_dwordx4 v229, s[62:63] offset:1024\n\t"
      "v_exp_f32 v128, v128\n\t"
      "v_exp_f32 v129, v129\n\t"
      "v_smfmac_f32_32x32x32_bf16 v[48:63], a[24:27], v[152:159], v224\n\t"
      "v_exp_f32 v130, v130\n\t"
      "v_exp_f32 v131, v131\n\t"
      "v_exp_f32 v132, v132\n\t"
      "v_smfmac_f32_16x16x64_bf16 v[220:223], a[32:35], v[152:159], v224\n\t"
      "ds_read_b128 a[0:3], v225 offset:8192\n\t"
      "v_exp_f32 v133, v133\n\t"
      "v_exp_f32 v134, v134\n\t"
      "v_mfma_f32_32x32x16_bf16 v[144:159], a[8:11], v[184:187], v[80:95]\n\t"
      "v_exp_f32 v135, v135\n\t"
      "v_exp_f32 v136, v136\n\t"
      "v_exp_f32 v137, v137\n\t"
      "v_mfma_f32_32x32x16_bf16 v[144:159], a[12:15], v[188:191], v[144:159]\n\t"
      "s_waitcnt lgkmcnt(3)\n\t"
      "v_exp_f32 v138, v138\n\t"
      "v_exp_f32 v139, v139\n\t"
      "v_exp_f32 v140, v140\n\t"
      "v_smfmac_f32_32x32x32_bf16 v[0:15], a[20:23], v[128:135], v224\n\t"
      "s_nop 1\n\t"
      "v_exp_f32 v141, v141\n\t"
      "v_exp_f32 v142, v142\n\t"
      "v_exp_f32 v143, v143\n\t"
      "v_smfmac_f32_16x16x64_bf16 v[208:211], a[32:35], v[128:135], v224\n\t"
      "s_waitcnt lgkmcnt(1)\n\t"
      "ds_read_b128 a[4:7], v226 offset:8192\n\t"
      "v_exp_f32 v144, v144\n\t"
      "v_exp_f32 v145, v145\n\t"
      "v_smfmac_f32_32x32x32_bf16 v[0:15], a[28:31], v[136:143], v224\n\t"
      "v_exp_f32 v146, v146\n\t"
      "v_exp_f32 v147, v147\n\t"
      "v_exp_f32 v148, v148\n\t"
      "v_smfmac_f32_16x16x64_bf16 v[208:211], a[32:35], v[136:143], v224\n\t"
      "ds_read_b64_tr_b16 a[16:17], v227 offset:8192\n\t"
      "v_exp_f32 v149, v149\n\t"
      "v_exp_f32 v150, v150\n\t"
      "v_mfma_f32_32x32x16_bf16 v[128:143], a[8:11], v[192:195], v[96:111]\n\t"
      "v_exp_f32 v151, v151\n\t"
      "v_exp_f32 v152, v152\n\t"
      "v_exp_f32 v153, v153\n\t"
      "v_mfma_f32_32x32x16_bf16 v[128:143], a[12:15], v[196:199], v[128:143]\n\t"
      "s_nop 0\n\t"
      "v_exp_f32 v154, v154\n\t"
      "v_exp_f32 v155, v155\n\t"
      "v_exp_f32 v156, v156\n\t"
      "v_smfmac_f32_32x32x32_bf16 v[16:31], a[20:23], v[144:151], v224\n\t"
      "s_nop 1\n\t"
      "v_exp_f32 v157, v157\n\t"
      "v_exp_f32 v158, v158\n\t"
      "v_exp_f32 v159, v159\n\t"
      "v_smfmac_f32_16x16x64_bf16 v[212:215], a[32:35], v[144:151], v224\n\t"
      "ds_read_b64_tr_b16 a[18:19], v227 offset:8448\n\t"
      "v_exp_f32 v128, v128\n\t"
      "v_exp_f32 v129, v129\n\t"
      "v_smfmac_f32_32x32x32_bf16 v[16:31], a[28:31], v[152:159], v224\n\t"
      "v_exp_f32 v130, v130\n\t"
      "v_exp_f32 v131, v131\n\t"
      "v_exp_f32 v132, v132\n\t"
      "v_smfmac_f32_16x16x64_bf16 v[212:215], a[32:35], v[152:159], v224\n\t"
      "ds_read_b64_tr_b16 a[24:25], v227 offset:9216\n\t"
      "v_exp_f32 v133, v133\n\t"
      "v_exp_f32 v134, v134\n\t"
      "v_mfma_f32_32x32x16_bf16 v[144:159], a[8:11], v[200:203], v[112:127]\n\t"
      "v_exp_f32 v135, v135\n\t"
      "v_exp_f32 v136, v136\n\t"
      "v_exp_f32 v137, v137\n\t"
      "v_mfma_f32_32x32x16_bf16 v[144:159], a[12:15], v[204:207], v[144:159]\n\t"
      "s_nop 0\n\t"
      "v_exp_f32 v138, v138\n\t"
      "v_exp_f32 v139, v139\n\t"
      "v_exp_f32 v140, v140\n\t"
      "v_smfmac_f32_32x32x32_bf16 v[32:47], a[20:23], v[128:135], v224\n\t"
      "s_nop 1\n\t"
      "v_exp_f32 v141, v141\n\t"
      "v_exp_f32 v142, v142\n\t"
      "v_exp_f32 v143, v143\n\t"
      "v_smfmac_f32_16x16x64_bf16 v[216:219], a[32:35], v[128:135], v224\n\t"
      "ds_read_b64_tr_b16 a[26:27], v227 offset:9472\n\t"
      "v_exp_f32 v144, v144\n\t"
      "v_exp_f32 v145, v145\n\t"
      "v_smfmac_f32_32x32x32_bf16 v[32:47], a[28:31], v[136:143], v224\n\t"
      "v_exp_f32 v146, v146\n\t"
      "v_exp_f32 v147, v147\n\t"
      "v_exp_f32 v148, v148\n\t"
      "v_smfmac_f32_16x16x64_bf16 v[216:219], a[32:35], v[136:143], v224\n\t"
      "s_waitcnt lgkmcnt(4)\n\t"
      "global_load_lds_dwordx4 v229, s[62:63] offset:2048\n\t"
      "v_exp_f32 v149, v149\n\t"
      "v_exp_f32 v150, v150\n\t"
      "v_mfma_f32_32x32x16_bf16 v[128:143], a[0:3], v[176:179], v[64:79]\n\t"
      "v_exp_f32 v151, v151\n\t"
      "v_exp_f32 v152, v152\n\t"
      "v_exp_f32 v153, v153\n\t"
      "v_mfma_f32_32x32x16_bf16 v[128:143], a[4:7], v[180:183], v[128:143]\n\t"
      "s_nop 0\n\t"
      "v_exp_f32 v154, v154\n\t"
      "v_exp_f32 v155, v155\n\t"
      "v_exp_f32 v156, v156\n\t"
      "v_smfmac_f32_32x32x32_bf16 v[48:63], a[20:23], v[144:151], v224\n\t"
      "s_nop 1\n\t"
      "v_exp_f32 v157, v157\n\t"
      "v_exp_f32 v158, v158\n\t"
      "v_exp_f32 v159, v159\n\t"
      "v_smfmac_f32_16x16x64_bf16 v[220:223], a[32:35], v[144:151], v224\n\t"
      "global_load_lds_dwordx4 v229, s[62:63] offset:3072\n\t"
      "v_exp_f32 v128, v128\n\t"
      "v_exp_f32 v129, v129\n\t"
      "v_smfmac_f32_32x32x32_bf16 v[48:63], a[28:31], v[152:159], v224\n\t"
      "s_add_u32 s62, s62, 0x4000\n\t"
      "s_addc_u32 s63, s63, 0\n\t"
      "v_exp_f32 v130, v130\n\t"
      "v_exp_f32 v131, v131\n\t"
      "v_exp_f32 v132, v132\n\t"
      "v_smfmac_f32_16x16x64_bf16 v[220:223], a[32:35], v[152:159], v224\n\t"
      "ds_read_b128 a[8:11], v225 offset:10240\n\t"
      "v_exp_f32 v133, v133\n\t"
      "v_exp_f32 v134, v134\n\t"
      "v_mfma_f32_32x32x16_bf16 v[144:159], a[0:3], v[184:187], v[80:95]\n\t"
      "v_exp_f32 v135, v135\n\t"
      "v_exp_f32 v136, v136\n\t"
      "v_exp_f32 v137, v137\n\t"
      "v_mfma_f32_32x32x16_bf16 v[144:159], a[4:7], v[188:191], v[144:159]\n\t"
      "s_waitcnt lgkmcnt(3)\n\t"
      "v_exp_f32 v138, v138\n\t"
      "v_exp_f32 v139, v139\n\t"
      "v_exp_f32 v140, v140\n\t"
      "v_smfmac_f32_32x32x32_bf16 v[0:15], a[16:19], v[128:135], v224\n\t"
      "s_nop 1\n\t"
      "v_exp_f32 v141, v141\n\t"
      "v_exp_f32 v142, v142\n\t"
      "v_exp_f32 v143, v143\n\t"
      "v_smfmac_f32_16x16x64_bf16 v[208:211], a[32:35], v[128:135], v224\n\t"
      "s_waitcnt lgkmcnt(1)\n\t"
      "ds_read_b128 a[12:15], v226 offset:10240\n\t"
      "v_exp_f32 v144, v144\n\t"
      "v_exp_f32 v145, v145\n\t"
      "v_smfmac_f32_32x32x32_bf16 v[0:15], a[24:27], v[136:143], v224\n\t"
      "s_cmp_eq_u32 s64, 5\n\t"
      "s_cselect_b64 s[60:61], %[kptr], s[60:61]\n\t"
      "v_exp_f32 v146, v146\n\t"
      "v_exp_f32 v147, v147\n\t"
      "v_exp_f32 v148, v148\n\t"
      "v_smfmac_f32_16x16x64_bf16 v[208:211], a[32:35], v[136:143], v224\n\t"
      "ds_read_b64_tr_b16 a[20:21], v227 offset:10240\n\t"
      "v_exp_f32 v149, v149\n\t"
      "v_exp_f32 v150, v150\n\t"
      "v_mfma_f32_32x32x16_bf16 v[128:143], a[0:3], v[192:195], v[96:111]\n\t"
      "s_cselect_b64 s[62:63], %[vptr], s[62:63]\n\t"
      "v_exp_f32 v151, v151\n\t"
      "v_exp_f32 v152, v152\n\t"
      "v_exp_f32 v153, v153\n\t"
      "v_mfma_f32_32x32x16_bf16 v[128:143], a[4:7], v[196:199], v[128:143]\n\t"
      "s_nop 0\n\t"
      "v_exp_f32 v154, v154\n\t"
      "v_exp_f32 v155, v155\n\t"
      "v_exp_f32 v156, v156\n\t"
      "v_smfmac_f32_32x32x32_bf16 v[16:31], a[16:19], v[144:151], v224\n\t"
      "s_nop 1\n\t"
      "v_exp_f32 v157, v157\n\t"
      "v_exp_f32 v158, v158\n\t"
      "v_exp_f32 v159, v159\n\t"
      "v_smfmac_f32_16x16x64_bf16 v[212:215], a[32:35], v[144:151], v224\n\t"
      "ds_read_b64_tr_b16 a[22:23], v227 offset:10496\n\t"
      "v_exp_f32 v128, v128\n\t"
      "v_exp_f32 v129, v129\n\t"
      "v_smfmac_f32_32x32x32_bf16 v[16:31], a[24:27], v[152:159], v224\n\t"
      "v_exp_f32 v130, v130\n\t"
      "v_exp_f32 v131, v131\n\t"
      "v_exp_f32 v132, v132\n\t"
      "v_smfmac_f32_16x16x64_bf16 v[212:215], a[32:35], v[152:159], v224\n\t"
      "ds_read_b64_tr_b16 a[28:29], v227 offset:11264\n\t"
      "v_exp_f32 v133, v133\n\t"
      "v_exp_f32 v134, v134\n\t"
      "v_mfma_f32_32x32x16_bf16 v[144:159], a[0:3], v[200:203], v[112:127]\n\t"
      "v_exp_f32 v135, v135\n\t"
      "v_exp_f32 v136, v136\n\t"
      "v_exp_f32 v137, v137\n\t"
      "v_mfma_f32_32x32x16_bf16 v[144:159], a[4:7], v[204:207], v[144:159]\n\t"
      "s_nop 0\n\t"
      "v_exp_f32 v138, v138\n\t"
      "v_exp_f32 v139, v139\n\t"
      "v_exp_f32 v140, v140\n\t"
      "v_smfmac_f32_32x32x32_bf16 v[32:47], a[16:19], v[128:135], v224\n\t"
      "s_nop 1\n\t"
      "v_exp_f32 v141, v141\n\t"
      "v_exp_f32 v142, v142\n\t"
      "v_exp_f32 v143, v143\n\t"
      "v_smfmac_f32_16x16x64_bf16 v[216:219], a[32:35], v[128:135], v224\n\t"
      "ds_read_b64_tr_b16 a[30:31], v227 offset:11520\n\t"
      "v_exp_f32 v144, v144\n\t"
      "v_exp_f32 v145, v145\n\t"
      "v_smfmac_f32_32x32x32_bf16 v[32:47], a[24:27], v[136:143], v224\n\t"
      "v_exp_f32 v146, v146\n\t"
      "v_exp_f32 v147, v147\n\t"
      "v_exp_f32 v148, v148\n\t"
      "v_smfmac_f32_16x16x64_bf16 v[216:219], a[32:35], v[136:143], v224\n\t"
      "s_waitcnt lgkmcnt(4)\n\t"
      "v_exp_f32 v149, v149\n\t"
      "v_exp_f32 v150, v150\n\t"
      "v_mfma_f32_32x32x16_bf16 v[128:143], a[8:11], v[176:179], v[64:79]\n\t"
      "v_exp_f32 v151, v151\n\t"
      "v_exp_f32 v152, v152\n\t"
      "v_exp_f32 v153, v153\n\t"
      "v_mfma_f32_32x32x16_bf16 v[128:143], a[12:15], v[180:183], v[128:143]\n\t"
      "s_nop 0\n\t"
      "v_exp_f32 v154, v154\n\t"
      "v_exp_f32 v155, v155\n\t"
      "v_exp_f32 v156, v156\n\t"
      "v_smfmac_f32_32x32x32_bf16 v[48:63], a[16:19], v[144:151], v224\n\t"
      "s_nop 1\n\t"
      "v_exp_f32 v157, v157\n\t"
      "v_exp_f32 v158, v158\n\t"
      "v_exp_f32 v159, v159\n\t"
      "v_smfmac_f32_16x16x64_bf16 v[220:223], a[32:35], v[144:151], v224\n\t"
      "s_nop 0\n\t"
      "v_exp_f32 v128, v128\n\t"
      "v_exp_f32 v129, v129\n\t"
      "v_smfmac_f32_32x32x32_bf16 v[48:63], a[24:27], v[152:159], v224\n\t"
      "s_add_u32 s66, s64, 3\n\t"
      "s_and_b32 s66, s66, 3\n\t"
      "v_exp_f32 v130, v130\n\t"
      "v_exp_f32 v131, v131\n\t"
      "v_exp_f32 v132, v132\n\t"
      "v_smfmac_f32_16x16x64_bf16 v[220:223], a[32:35], v[152:159], v224\n\t"
      "ds_read_b128 a[0:3], v225 offset:12288\n\t"
      "v_exp_f32 v133, v133\n\t"
      "v_exp_f32 v134, v134\n\t"
      "v_mfma_f32_32x32x16_bf16 v[144:159], a[8:11], v[184:187], v[80:95]\n\t"
      "v_exp_f32 v135, v135\n\t"
      "v_exp_f32 v136, v136\n\t"
      "v_exp_f32 v137, v137\n\t"
      "v_mfma_f32_32x32x16_bf16 v[144:159], a[12:15], v[188:191], v[144:159]\n\t"
      "s_waitcnt lgkmcnt(3)\n\t"
      "v_exp_f32 v138, v138\n\t"
      "v_exp_f32 v139, v139\n\t"
      "v_exp_f32 v140, v140\n\t"
      "v_smfmac_f32_32x32x32_bf16 v[0:15], a[20:23], v[128:135], v224\n\t"
      "s_nop 1\n\t"
      "v_exp_f32 v141, v141\n\t"
      "v_exp_f32 v142, v142\n\t"
      "v_exp_f32 v143, v143\n\t"
      "v_smfmac_f32_16x16x64_bf16 v[208:211], a[32:35], v[128:135], v224\n\t"
      "s_waitcnt lgkmcnt(1)\n\t"
      "ds_read_b128 a[4:7], v226 offset:12288\n\t"
      "v_exp_f32 v144, v144\n\t"
      "v_exp_f32 v145, v145\n\t"
      "v_smfmac_f32_32x32x32_bf16 v[0:15], a[28:31], v[136:143], v224\n\t"
      "s_lshl_b32 s66, s66, 15\n\t"
      "s_add_u32 s66, s66, %[dst0]\n\t"
      "v_exp_f32 v146, v146\n\t"
      "v_exp_f32 v147, v147\n\t"
      "v_exp_f32 v148, v148\n\t"
      "v_smfmac_f32_16x16x64_bf16 v[208:211], a[32:35], v[136:143], v224\n\t"
      "ds_read_b64_tr_b16 a[16:17], v227 offset:12288\n\t"
      "v_exp_f32 v149, v149\n\t"
      "v_exp_f32 v150, v150\n\t"
      "v_mfma_f32_32x32x16_bf16 v[128:143], a[8:11], v[192:195], v[96:111]\n\t"
      "v_exp_f32 v151, v151\n\t"
      "v_exp_f32 v152, v152\n\t"
      "v_exp_f32 v153, v153\n\t"
      "v_mfma_f32_32x32x16_bf16 v[128:143], a[12:15], v[196:199], v[128:143]\n\t"
      "s_nop 0\n\t"
      "v_exp_f32 v154, v154\n\t"
      "v_exp_f32 v155, v155\n\t"
      "v_exp_f32 v156, v156\n\t"
      "v_smfmac_f32_32x32x32_bf16 v[16:31], a[20:23], v[144:151], v224\n\t"
      "s_nop 1\n\t"
      "v_exp_f32 v157, v157\n\t"
      "v_exp_f32 v158, v158\n\t"
      "v_exp_f32 v159, v159\n\t"
      "v_smfmac_f32_16x16x64_bf16 v[212:215], a[32:35], v[144:151], v224\n\t"
      "ds_read_b64_tr_b16 a[18:19], v227 offset:12544\n\t"
      "v_exp_f32 v128, v128\n\t"
      "v_exp_f32 v129, v129\n\t"
      "v_smfmac_f32_32x32x32_bf16 v[16:31], a[28:31], v[152:159], v224\n\t"
      "s_add_u32 s67, s66, 0x4000\n\t"
      "v_exp_f32 v130, v130\n\t"
      "v_exp_f32 v131, v131\n\t"
      "v_exp_f32 v132, v132\n\t"
      "v_smfmac_f32_16x16x64_bf16 v[212:215], a[32:35], v[152:159], v224\n\t"
      "ds_read_b64_tr_b16 a[24:25], v227 offset:13312\n\t"
      "v_exp_f32 v133, v133\n\t"
      "v_exp_f32 v134, v134\n\t"
      "v_mfma_f32_32x32x16_bf16 v[144:159], a[8:11], v[200:203], v[112:127]\n\t"
      "v_exp_f32 v135, v135\n\t"
      "v_exp_f32 v136, v136\n\t"
      "v_exp_f32 v137, v137\n\t"
      "v_mfma_f32_32x32x16_bf16 v[144:159], a[12:15], v[204:207], v[144:159]\n\t"
      "s_nop 0\n\t"
      "v_exp_f32 v138, v138\n\t"
      "v_exp_f32 v139, v139\n\t"
      "v_exp_f32 v140, v140\n\t"
      "v_smfmac_f32_32x32x32_bf16 v[32:47], a[20:23], v[128:135], v224\n\t"
      "s_nop 1\n\t"
      "v_exp_f32 v141, v141\n\t"
      "v_exp_f32 v142, v142\n\t"
      "v_exp_f32 v143, v143\n\t"
      "v_smfmac_f32_16x16x64_bf16 v[216:219], a[32:35], v[128:135], v224\n\t"
      "ds_read_b64_tr_b16 a[26:27], v227 offset:13568\n\t"
      "v_exp_f32 v144, v144\n\t"
      "v_exp_f32 v145, v145\n\t"
      "v_smfmac_f32_32x32x32_bf16 v[32:47], a[28:31], v[136:143], v224\n\t"
      "v_exp_f32 v146, v146\n\t"
      "v_exp_f32 v147, v147\n\t"
      "v_exp_f32 v148, v148\n\t"
      "v_smfmac_f32_16x16x64_bf16 v[216:219], a[32:35], v[136:143], v224\n\t"
      "s_waitcnt lgkmcnt(4)\n\t"
      "v_exp_f32 v149, v149\n\t"
      "v_exp_f32 v150, v150\n\t"
      "v_mfma_f32_32x32x16_bf16 v[128:143], a[0:3], v[176:179], v[64:79]\n\t"
      "v_exp_f32 v151, v151\n\t"
      "v_exp_f32 v152, v152\n\t"
      "v_exp_f32 v153, v153\n\t"
      "v_mfma_f32_32x32x16_bf16 v[128:143], a[4:7], v[180:183], v[128:143]\n\t"
      "s_nop 0\n\t"
      "v_exp_f32 v154, v154\n\t"
      "v_exp_f32 v155, v155\n\t"
      "v_exp_f32 v156, v156\n\t"
      "v_smfmac_f32_32x32x32_bf16 v[48:63], a[20:23], v[144:151], v224\n\t"
      "s_nop 1\n\t"
      "v_exp_f32 v157, v157\n\t"
      "v_exp_f32 v158, v158\n\t"
      "v_exp_f32 v159, v159\n\t"
      "v_smfmac_f32_16x16x64_bf16 v[220:223], a[32:35], v[144:151], v224\n\t"
      "s_nop 0\n\t"
      "v_exp_f32 v128, v128\n\t"
      "v_exp_f32 v129, v129\n\t"
      "v_smfmac_f32_32x32x32_bf16 v[48:63], a[28:31], v[152:159], v224\n\t"
      "v_exp_f32 v130, v130\n\t"
      "v_exp_f32 v131, v131\n\t"
      "v_exp_f32 v132, v132\n\t"
      "v_smfmac_f32_16x16x64_bf16 v[220:223], a[32:35], v[152:159], v224\n\t"
      "ds_read_b128 a[8:11], v225 offset:14336\n\t"
      "v_exp_f32 v133, v133\n\t"
      "v_exp_f32 v134, v134\n\t"
      "v_mfma_f32_32x32x16_bf16 v[144:159], a[0:3], v[184:187], v[80:95]\n\t"
      "v_exp_f32 v135, v135\n\t"
      "v_exp_f32 v136, v136\n\t"
      "v_exp_f32 v137, v137\n\t"
      "v_mfma_f32_32x32x16_bf16 v[144:159], a[4:7], v[188:191], v[144:159]\n\t"
      "s_waitcnt lgkmcnt(3)\n\t"
      "v_exp_f32 v138, v138\n\t"
      "v_exp_f32 v139, v139\n\t"
      "v_exp_f32 v140, v140\n\t"
      "v_smfmac_f32_32x32x32_bf16 v[0:15], a[16:19], v[128:135], v224\n\t"
      "s_nop 1\n\t"
      "v_exp_f32 v141, v141\n\t"
      "v_exp_f32 v142, v142\n\t"
      "v_exp_f32 v143, v143\n\t"
      "v_smfmac_f32_16x16x64_bf16 v[208:211], a[32:35], v[128:135], v224\n\t"
      "s_waitcnt lgkmcnt(1)\n\t"
      "ds_read_b128 a[12:15], v226 offset:14336\n\t"
      "v_exp_f32 v144, v144\n\t"
      "v_exp_f32 v145, v145\n\t"
      "v_smfmac_f32_32x32x32_bf16 v[0:15], a[24:27], v[136:143], v224\n\t"
      "v_exp_f32 v146, v146\n\t"
      "v_exp_f32 v147, v147\n\t"
      "v_exp_f32 v148, v148\n\t"
      "v_smfmac_f32_16x16x64_bf16 v[208:211], a[32:35], v[136:143], v224\n\t"
      "ds_read_b64_tr_b16 a[20:21], v227 offset:14336\n\t"
      "v_exp_f32 v149, v149\n\t"
      "v_exp_f32 v150, v150\n\t"
      "v_mfma_f32_32x32x16_bf16 v[128:143], a[0:3], v[192:195], v[96:111]\n\t"
      "v_exp_f32 v151, v151\n\t"
      "v_exp_f32 v152, v152\n\t"
      "v_exp_f32 v153, v153\n\t"
      "v_mfma_f32_32x32x16_bf16 v[128:143], a[4:7], v[196:199], v[128:143]\n\t"
      "s_nop 0\n\t"
      "v_exp_f32 v154, v154\n\t"
      "v_exp_f32 v155, v155\n\t"
      "v_exp_f32 v156, v156\n\t"
      "v_smfmac_f32_32x32x32_bf16 v[16:31], a[16:19], v[144:151], v224\n\t"
      "s_nop 1\n\t"
      "v_exp_f32 v157, v157\n\t"
      "v_exp_f32 v158, v158\n\t"
      "v_exp_f32 v159, v159\n\t"
      "v_smfmac_f32_16x16x64_bf16 v[212:215], a[32:35], v[144:151], v224\n\t"
      "ds_read_b64_tr_b16 a[22:23], v227 offset:14592\n\t"
      "v_exp_f32 v128, v128\n\t"
      "v_exp_f32 v129, v129\n\t"
      "v_smfmac_f32_32x32x32_bf16 v[16:31], a[24:27], v[152:159], v224\n\t"
      "s_add_u32 s64, s64, 1\n\t"
      "s_and_b32 s65, s64, 3\n\t"
      "v_exp_f32 v130, v130\n\t"
      "v_exp_f32 v131, v131\n\t"
      "v_exp_f32 v132, v132\n\t"
      "v_smfmac_f32_16x16x64_bf16 v[212:215], a[32:35], v[152:159], v224\n\t"
      "ds_read_b64_tr_b16 a[28:29], v227 offset:15360\n\t"
      "v_exp_f32 v133, v133\n\t"
      "v_exp_f32 v134, v134\n\t"
      "v_mfma_f32_32x32x16_bf16 v[144:159], a[0:3], v[200:203], v[112:127]\n\t"
      "v_exp_f32 v135, v135\n\t"
      "v_exp_f32 v136, v136\n\t"
      "v_exp_f32 v137, v137\n\t"
      "v_mfma_f32_32x32x16_bf16 v[144:159], a[4:7], v[204:207], v[144:159]\n\t"
      "s_nop 0\n\t"
      "v_exp_f32 v138, v138\n\t"
      "v_exp_f32 v139, v139\n\t"
      "v_exp_f32 v140, v140\n\t"
      "v_smfmac_f32_32x32x32_bf16 v[32:47], a[16:19], v[128:135], v224\n\t"
      "s_nop 1\n\t"
      "v_exp_f32 v141, v141\n\t"
      "v_exp_f32 v142, v142\n\t"
      "v_exp_f32 v143, v143\n\t"
      "v_smfmac_f32_16x16x64_bf16 v[216:219], a[32:35], v[128:135], v224\n\t"
      "ds_read_b64_tr_b16 a[30:31], v227 offset:15616\n\t"
      "v_exp_f32 v144, v144\n\t"
      "v_exp_f32 v145, v145\n\t"
      "v_smfmac_f32_32x32x32_bf16 v[32:47], a[24:27], v[136:143], v224\n\t"
      "s_lshl_b32 s65, s65, 15\n\t"
      "v_exp_f32 v146, v146\n\t"
      "v_exp_f32 v147, v147\n\t"
      "v_exp_f32 v148, v148\n\t"
      "v_smfmac_f32_16x16x64_bf16 v[216:219], a[32:35], v[136:143], v224\n\t"
      "s_waitcnt lgkmcnt(4)\n\t"
      "v_exp_f32 v149, v149\n\t"
      "v_exp_f32 v150, v150\n\t"
      "v_mfma_f32_32x32x16_bf16 v[128:143], a[8:11], v[176:179], v[64:79]\n\t"
      "v_exp_f32 v151, v151\n\t"
      "v_exp_f32 v152, v152\n\t"
      "v_exp_f32 v153, v153\n\t"
      "v_mfma_f32_32x32x16_bf16 v[128:143], a[12:15], v[180:183], v[128:143]\n\t"
      "s_nop 0\n\t"
      "v_exp_f32 v154, v154\n\t"
      "v_exp_f32 v155, v155\n\t"
      "v_exp_f32 v156, v156\n\t"
      "v_smfmac_f32_32x32x32_bf16 v[48:63], a[16:19], v[144:151], v224\n\t"
      "s_waitcnt vmcnt(8)\n\t"
      "s_barrier\n\t"
      "v_exp_f32 v157, v157\n\t"
      "v_exp_f32 v158, v158\n\t"
      "v_exp_f32 v159, v159\n\t"
      "v_smfmac_f32_16x16x64_bf16 v[220:223], a[32:35], v[144:151], v224\n\t"
      "s_nop 0\n\t"
      "v_add_u32 v225, s65, v230\n\t"
      "v_add_u32 v226, s65, v231\n\t"
      "v_add_u32 v227, s65, v232\n\t"
      "v_exp_f32 v128, v128\n\t"
      "v_exp_f32 v129, v129\n\t"
      "v_smfmac_f32_32x32x32_bf16 v[48:63], a[24:27], v[152:159], v224\n\t"
      "v_exp_f32 v130, v130\n\t"
      "v_exp_f32 v131, v131\n\t"
      "v_exp_f32 v132, v132\n\t"
      "v_smfmac_f32_16x16x64_bf16 v[220:223], a[32:35], v[152:159], v224\n\t"
      "ds_read_b128 a[0:3], v225 offset:0\n\t"
      "v_exp_f32 v133, v133\n\t"
      "v_exp_f32 v134, v134\n\t"
      "v_mfma_f32_32x32x16_bf16 v[144:159], a[8:11], v[184:187], v[80:95]\n\t"
      "v_exp_f32 v135, v135\n\t"
      "v_exp_f32 v136, v136\n\t"
      "v_exp_f32 v137, v137\n\t"
      "v_mfma_f32_32x32x16_bf16 v[144:159], a[12:15], v[188:191], v[144:159]\n\t"
      "s_waitcnt lgkmcnt(3)\n\t"
      "v_exp_f32 v138, v138\n\t"
      "v_exp_f32 v139, v139\n\t"
      "v_exp_f32 v140, v140\n\t"
      "v_smfmac_f32_32x32x32_bf16 v[0:15], a[20:23], v[128:135], v224\n\t"
      "s_nop 1\n\t"
      "v_exp_f32 v141, v141\n\t"
      "v_exp_f32 v142, v142\n\t"
      "v_exp_f32 v143, v143\n\t"
      "v_smfmac_f32_16x16x64_bf16 v[208:211], a[32:35], v[128:135], v224\n\t"
      "s_waitcnt lgkmcnt(1)\n\t"
      "ds_read_b128 a[4:7], v226 offset:0\n\t"
      "v_exp_f32 v144, v144\n\t"
      "v_exp_f32 v145, v145\n\t"
      "v_smfmac_f32_32x32x32_bf16 v[0:15], a[28:31], v[136:143], v224\n\t"
      "v_exp_f32 v146, v146\n\t"
      "v_exp_f32 v147, v147\n\t"
      "v_exp_f32 v148, v148\n\t"
      "v_smfmac_f32_16x16x64_bf16 v[208:211], a[32:35], v[136:143], v224\n\t"
      "ds_read_b64_tr_b16 a[16:17], v227 offset:0\n\t"
      "v_exp_f32 v149, v149\n\t"
      "v_exp_f32 v150, v150\n\t"
      "v_mfma_f32_32x32x16_bf16 v[128:143], a[8:11], v[192:195], v[96:111]\n\t"
      "v_exp_f32 v151, v151\n\t"
      "v_exp_f32 v152, v152\n\t"
      "v_exp_f32 v153, v153\n\t"
      "v_mfma_f32_32x32x16_bf16 v[128:143], a[12:15], v[196:199], v[128:143]\n\t"
      "s_nop 0\n\t"
      "v_exp_f32 v154, v154\n\t"
      "v_exp_f32 v155, v155\n\t"
      "v_exp_f32 v156, v156\n\t"
      "v_smfmac_f32_32x32x32_bf16 v[16:31], a[20:23], v[144:151], v224\n\t"
      "s_nop 1\n\t"
      "v_exp_f32 v157, v157\n\t"
      "v_exp_f32 v158, v158\n\t"
      "v_exp_f32 v159, v159\n\t"
      "v_smfmac_f32_16x16x64_bf16 v[212:215], a[32:35], v[144:151], v224\n\t"
      "ds_read_b64_tr_b16 a[18:19], v227 offset:256\n\t"
      "v_exp_f32 v128, v128\n\t"
      "v_exp_f32 v129, v129\n\t"
      "v_smfmac_f32_32x32x32_bf16 v[16:31], a[28:31], v[152:159], v224\n\t"
      "v_exp_f32 v130, v130\n\t"
      "v_exp_f32 v131, v131\n\t"
      "v_exp_f32 v132, v132\n\t"
      "v_smfmac_f32_16x16x64_bf16 v[212:215], a[32:35], v[152:159], v224\n\t"
      "ds_read_b64_tr_b16 a[24:25], v227 offset:1024\n\t"
      "v_exp_f32 v133, v133\n\t"
      "v_exp_f32 v134, v134\n\t"
      "v_mfma_f32_32x32x16_bf16 v[144:159], a[8:11], v[200:203], v[112:127]\n\t"
      "v_exp_f32 v135, v135\n\t"
      "v_exp_f32 v136, v136\n\t"
      "v_exp_f32 v137, v137\n\t"
      "v_mfma_f32_32x32x16_bf16 v[144:159], a[12:15], v[204:207], v[144:159]\n\t"
      "s_nop 0\n\t"
      "v_exp_f32 v138, v138\n\t"
      "v_exp_f32 v139, v139\n\t"
      "v_exp_f32 v140, v140\n\t"
      "v_smfmac_f32_32x32x32_bf16 v[32:47], a[20:23], v[128:135], v224\n\t"
      "s_nop 1\n\t"
      "v_exp_f32 v141, v141\n\t"
      "v_exp_f32 v142, v142\n\t"
      "v_exp_f32 v143, v143\n\t"
      "v_smfmac_f32_16x16x64_bf16 v[216:219], a[32:35], v[128:135], v224\n\t"
      "ds_read_b64_tr_b16 a[26:27], v227 offset:1280\n\t"
      "v_exp_f32 v144, v144\n\t"
      "v_exp_f32 v145, v145\n\t"
      "v_smfmac_f32_32x32x32_bf16 v[32:47], a[28:31], v[136:143], v224\n\t"
      "v_exp_f32 v146, v146\n\t"
      "v_exp_f32 v147, v147\n\t"
      "v_exp_f32 v148, v148\n\t"
      "v_smfmac_f32_16x16x64_bf16 v[216:219], a[32:35], v[136:143], v224\n\t"
      "s_waitcnt lgkmcnt(4)\n\t"
      "v_exp_f32 v149, v149\n\t"
      "v_exp_f32 v150, v150\n\t"
      "v_mfma_f32_32x32x16_bf16 v[128:143], a[0:3], v[176:179], v[64:79]\n\t"
      "v_exp_f32 v151, v151\n\t"
      "v_exp_f32 v152, v152\n\t"
      "v_exp_f32 v153, v153\n\t"
      "v_mfma_f32_32x32x16_bf16 v[128:143], a[4:7], v[180:183], v[128:143]\n\t"
      "s_nop 0\n\t"
      "v_exp_f32 v154, v154\n\t"
      "v_exp_f32 v155, v155\n\t"
      "v_exp_f32 v156, v156\n\t"
      "v_smfmac_f32_32x32x32_bf16 v[48:63], a[20:23], v[144:151], v224\n\t"
      "s_nop 1\n\t"
      "v_exp_f32 v157, v157\n\t"
      "v_exp_f32 v158, v158\n\t"
      "v_exp_f32 v159, v159\n\t"
      "v_smfmac_f32_16x16x64_bf16 v[220:223], a[32:35], v[144:151], v224\n\t"
      "s_cmp_lt_u32 s64, 7\n\t"
      "s_cbranch_scc1 1b\n\t"
      "v_add_u32 v234, 0x1000, v239\n\t"
      "v_exp_f32 v128, v128\n\t"
      "v_exp_f32 v129, v129\n\t"
      "v_smfmac_f32_32x32x32_bf16 v[48:63], a[28:31], v[152:159], v224\n\t"
      "v_exp_f32 v130, v130\n\t"
      "v_exp_f32 v131, v131\n\t"
      "v_exp_f32 v132, v132\n\t"
      "v_smfmac_f32_16x16x64_bf16 v[220:223], a[32:35], v[152:159], v224\n\t"
      "ds_read_b128 a[8:11], v225 offset:2048\n\t"
      "v_exp_f32 v133, v133\n\t"
      "v_exp_f32 v134, v134\n\t"
      "v_mfma_f32_32x32x16_bf16 v[144:159], a[0:3], v[184:187], v[80:95]\n\t"
      "v_exp_f32 v135, v135\n\t"
      "v_exp_f32 v136, v136\n\t"
      "v_exp_f32 v137, v137\n\t"
      "v_mfma_f32_32x32x16_bf16 v[144:159], a[4:7], v[188:191], v[144:159]\n\t"
      "s_waitcnt lgkmcnt(3)\n\t"
      "v_exp_f32 v138, v138\n\t"
      "v_exp_f32 v139, v139\n\t"
      "v_exp_f32 v140, v140\n\t"
      "v_smfmac_f32_32x32x32_bf16 v[0:15], a[16:19], v[128:135], v224\n\t"
      "s_nop 1\n\t"
      "v_exp_f32 v141, v141\n\t"
      "v_exp_f32 v142, v142\n\t"
      "v_exp_f32 v143, v143\n\t"
      "v_smfmac_f32_16x16x64_bf16 v[208:211], a[32:35], v[128:135], v224\n\t"
      "s_waitcnt lgkmcnt(1)\n\t"
      "ds_read_b128 a[12:15], v226 offset:2048\n\t"
      "v_exp_f32 v144, v144\n\t"
      "v_exp_f32 v145, v145\n\t"
      "v_smfmac_f32_32x32x32_bf16 v[0:15], a[24:27], v[136:143], v224\n\t"
      "v_exp_f32 v146, v146\n\t"
      "v_exp_f32 v147, v147\n\t"
      "v_exp_f32 v148, v148\n\t"
      "v_smfmac_f32_16x16x64_bf16 v[208:211], a[32:35], v[136:143], v224\n\t"
      "ds_read_b64_tr_b16 a[20:21], v227 offset:2048\n\t"
      "v_exp_f32 v149, v149\n\t"
      "v_exp_f32 v150, v150\n\t"
      "v_mfma_f32_32x32x16_bf16 v[128:143], a[0:3], v[192:195], v[96:111]\n\t"
      "v_exp_f32 v151, v151\n\t"
      "v_exp_f32 v152, v152\n\t"
      "v_exp_f32 v153, v153\n\t"
      "v_mfma_f32_32x32x16_bf16 v[128:143], a[4:7], v[196:199], v[128:143]\n\t"
      "s_nop 0\n\t"
      "v_exp_f32 v154, v154\n\t"
      "v_exp_f32 v155, v155\n\t"
      "v_exp_f32 v156, v156\n\t"
      "v_smfmac_f32_32x32x32_bf16 v[16:31], a[16:19], v[144:151], v224\n\t"
      "s_nop 1\n\t"
      "v_exp_f32 v157, v157\n\t"
      "v_exp_f32 v158, v158\n\t"
      "v_exp_f32 v159, v159\n\t"
      "v_smfmac_f32_16x16x64_bf16 v[212:215], a[32:35], v[144:151], v224\n\t"
      "ds_read_b64_tr_b16 a[22:23], v227 offset:2304\n\t"
      "v_exp_f32 v128, v128\n\t"
      "v_exp_f32 v129, v129\n\t"
      "v_smfmac_f32_32x32x32_bf16 v[16:31], a[24:27], v[152:159], v224\n\t"
      "v_exp_f32 v130, v130\n\t"
      "v_exp_f32 v131, v131\n\t"
      "v_exp_f32 v132, v132\n\t"
      "v_smfmac_f32_16x16x64_bf16 v[212:215], a[32:35], v[152:159], v224\n\t"
      "ds_read_b64_tr_b16 a[28:29], v227 offset:3072\n\t"
      "v_exp_f32 v133, v133\n\t"
      "v_exp_f32 v134, v134\n\t"
      "v_mfma_f32_32x32x16_bf16 v[144:159], a[0:3], v[200:203], v[112:127]\n\t"
      "v_exp_f32 v135, v135\n\t"
      "v_exp_f32 v136, v136\n\t"
      "v_exp_f32 v137, v137\n\t"
      "v_mfma_f32_32x32x16_bf16 v[144:159], a[4:7], v[204:207], v[144:159]\n\t"
      "s_nop 0\n\t"
      "v_exp_f32 v138, v138\n\t"
      "v_exp_f32 v139, v139\n\t"
      "v_exp_f32 v140, v140\n\t"
      "v_smfmac_f32_32x32x32_bf16 v[32:47], a[16:19], v[128:135], v224\n\t"
      "s_nop 1\n\t"
      "v_exp_f32 v141, v141\n\t"
      "v_exp_f32 v142, v142\n\t"
      "v_exp_f32 v143, v143\n\t"
      "v_smfmac_f32_16x16x64_bf16 v[216:219], a[32:35], v[128:135], v224\n\t"
      "ds_read_b64_tr_b16 a[30:31], v227 offset:3328\n\t"
      "v_exp_f32 v144, v144\n\t"
      "v_exp_f32 v145, v145\n\t"
      "v_smfmac_f32_32x32x32_bf16 v[32:47], a[24:27], v[136:143], v224\n\t"
      "v_exp_f32 v146, v146\n\t"
      "v_exp_f32 v147, v147\n\t"
      "v_exp_f32 v148, v148\n\t"
      "v_smfmac_f32_16x16x64_bf16 v[216:219], a[32:35], v[136:143], v224\n\t"
      "s_waitcnt lgkmcnt(4)\n\t"
      "v_exp_f32 v149, v149\n\t"
      "v_exp_f32 v150, v150\n\t"
      "v_mfma_f32_32x32x16_bf16 v[128:143], a[8:11], v[176:179], v[64:79]\n\t"
      "v_exp_f32 v151, v151\n\t"
      "v_exp_f32 v152, v152\n\t"
      "v_exp_f32 v153, v153\n\t"
      "v_mfma_f32_32x32x16_bf16 v[128:143], a[12:15], v[180:183], v[128:143]\n\t"
      "s_nop 0\n\t"
      "v_exp_f32 v154, v154\n\t"
      "v_exp_f32 v155, v155\n\t"
      "v_exp_f32 v156, v156\n\t"
      "v_smfmac_f32_32x32x32_bf16 v[48:63], a[16:19], v[144:151], v224\n\t"
      "s_nop 1\n\t"
      "v_exp_f32 v157, v157\n\t"
      "v_exp_f32 v158, v158\n\t"
      "v_exp_f32 v159, v159\n\t"
      "v_smfmac_f32_16x16x64_bf16 v[220:223], a[32:35], v[144:151], v224\n\t"
      "s_nop 0\n\t"
      "v_exp_f32 v128, v128\n\t"
      "v_exp_f32 v129, v129\n\t"
      "v_smfmac_f32_32x32x32_bf16 v[48:63], a[24:27], v[152:159], v224\n\t"
      "v_exp_f32 v130, v130\n\t"
      "v_exp_f32 v131, v131\n\t"
      "v_exp_f32 v132, v132\n\t"
      "v_smfmac_f32_16x16x64_bf16 v[220:223], a[32:35], v[152:159], v224\n\t"
      "ds_read_b128 a[0:3], v225 offset:4096\n\t"
      "v_exp_f32 v133, v133\n\t"
      "v_exp_f32 v134, v134\n\t"
      "v_mfma_f32_32x32x16_bf16 v[144:159], a[8:11], v[184:187], v[80:95]\n\t"
      "v_exp_f32 v135, v135\n\t"
      "v_exp_f32 v136, v136\n\t"
      "v_exp_f32 v137, v137\n\t"
      "v_mfma_f32_32x32x16_bf16 v[144:159], a[12:15], v[188:191], v[144:159]\n\t"
      "s_waitcnt lgkmcnt(3)\n\t"
      "v_exp_f32 v138, v138\n\t"
      "v_exp_f32 v139, v139\n\t"
      "v_exp_f32 v140, v140\n\t"
      "v_smfmac_f32_32x32x32_bf16 v[0:15], a[20:23], v[128:135], v224\n\t"
      "s_nop 1\n\t"
      "v_exp_f32 v141, v141\n\t"
      "v_exp_f32 v142, v142\n\t"
      "v_exp_f32 v143, v143\n\t"
      "v_smfmac_f32_16x16x64_bf16 v[208:211], a[32:35], v[128:135], v224\n\t"
      "s_waitcnt lgkmcnt(1)\n\t"
      "ds_read_b128 a[4:7], v226 offset:4096\n\t"
      "v_exp_f32 v144, v144\n\t"
      "v_exp_f32 v145, v145\n\t"
      "v_smfmac_f32_32x32x32_bf16 v[0:15], a[28:31], v[136:143], v224\n\t"
      "v_exp_f32 v146, v146\n\t"
      "v_exp_f32 v147, v147\n\t"
      "v_exp_f32 v148, v148\n\t"
      "v_smfmac_f32_16x16x64_bf16 v[208:211], a[32:35], v[136:143], v224\n\t"
      "ds_read_b64_tr_b16 a[16:17], v227 offset:4096\n\t"
      "v_exp_f32 v149, v149\n\t"
      "v_exp_f32 v150, v150\n\t"
      "v_mfma_f32_32x32x16_bf16 v[128:143], a[8:11], v[192:195], v[96:111]\n\t"
      "v_exp_f32 v151, v151\n\t"
      "v_exp_f32 v152, v152\n\t"
      "v_exp_f32 v153, v153\n\t"
      "v_mfma_f32_32x32x16_bf16 v[128:143], a[12:15], v[196:199], v[128:143]\n\t"
      "s_nop 0\n\t"
      "v_exp_f32 v154, v154\n\t"
      "v_exp_f32 v155, v155\n\t"
      "v_exp_f32 v156, v156\n\t"
      "v_smfmac_f32_32x32x32_bf16 v[16:31], a[20:23], v[144:151], v224\n\t"
      "s_nop 1\n\t"
      "v_exp_f32 v157, v157\n\t"
      "v_exp_f32 v158, v158\n\t"
      "v_exp_f32 v159, v159\n\t"
      "v_smfmac_f32_16x16x64_bf16 v[212:215], a[32:35], v[144:151], v224\n\t"
      "ds_read_b64_tr_b16 a[18:19], v227 offset:4352\n\t"
      "v_exp_f32 v128, v128\n\t"
      "v_exp_f32 v129, v129\n\t"
      "v_smfmac_f32_32x32x32_bf16 v[16:31], a[28:31], v[152:159], v224\n\t"
      "v_exp_f32 v130, v130\n\t"
      "v_exp_f32 v131, v131\n\t"
      "v_exp_f32 v132, v132\n\t"
      "v_smfmac_f32_16x16x64_bf16 v[212:215], a[32:35], v[152:159], v224\n\t"
      "ds_read_b64_tr_b16 a[24:25], v227 offset:5120\n\t"
      "v_exp_f32 v133, v133\n\t"
      "v_exp_f32 v134, v134\n\t"
      "v_mfma_f32_32x32x16_bf16 v[144:159], a[8:11], v[200:203], v[112:127]\n\t"
      "v_exp_f32 v135, v135\n\t"
      "v_exp_f32 v136, v136\n\t"
      "v_exp_f32 v137, v137\n\t"
      "v_mfma_f32_32x32x16_bf16 v[144:159], a[12:15], v[204:207], v[144:159]\n\t"
      "s_nop 0\n\t"
      "v_exp_f32 v138, v138\n\t"
      "v_exp_f32 v139, v139\n\t"
      "v_exp_f32 v140, v140\n\t"
      "v_smfmac_f32_32x32x32_bf16 v[32:47], a[20:23], v[128:135], v224\n\t"
      "s_nop 1\n\t"
      "v_exp_f32 v141, v141\n\t"
      "v_exp_f32 v142, v142\n\t"
      "v_exp_f32 v143, v143\n\t"
      "v_smfmac_f32_16x16x64_bf16 v[216:219], a[32:35], v[128:135], v224\n\t"
      "ds_read_b64_tr_b16 a[26:27], v227 offset:5376\n\t"
      "v_exp_f32 v144, v144\n\t"
      "v_exp_f32 v145, v145\n\t"
      "v_smfmac_f32_32x32x32_bf16 v[32:47], a[28:31], v[136:143], v224\n\t"
      "v_exp_f32 v146, v146\n\t"
      "v_exp_f32 v147, v147\n\t"
      "v_exp_f32 v148, v148\n\t"
      "v_smfmac_f32_16x16x64_bf16 v[216:219], a[32:35], v[136:143], v224\n\t"
      "s_waitcnt lgkmcnt(4)\n\t"
      "v_exp_f32 v149, v149\n\t"
      "v_exp_f32 v150, v150\n\t"
      "v_mfma_f32_32x32x16_bf16 v[128:143], a[0:3], v[176:179], v[64:79]\n\t"
      "v_exp_f32 v151, v151\n\t"
      "v_exp_f32 v152, v152\n\t"
      "v_exp_f32 v153, v153\n\t"
      "v_mfma_f32_32x32x16_bf16 v[128:143], a[4:7], v[180:183], v[128:143]\n\t"
      "s_nop 0\n\t"
      "v_exp_f32 v154, v154\n\t"
      "v_exp_f32 v155, v155\n\t"
      "v_exp_f32 v156, v156\n\t"
      "v_smfmac_f32_32x32x32_bf16 v[48:63], a[20:23], v[144:151], v224\n\t"
      "s_nop 1\n\t"
      "v_exp_f32 v157, v157\n\t"
      "v_exp_f32 v158, v158\n\t"
      "v_exp_f32 v159, v159\n\t"
      "v_smfmac_f32_16x16x64_bf16 v[220:223], a[32:35], v[144:151], v224\n\t"
      "s_nop 0\n\t"
      "v_exp_f32 v128, v128\n\t"
      "v_exp_f32 v129, v129\n\t"
      "v_smfmac_f32_32x32x32_bf16 v[48:63], a[28:31], v[152:159], v224\n\t"
      "v_exp_f32 v130, v130\n\t"
      "v_exp_f32 v131, v131\n\t"
      "v_exp_f32 v132, v132\n\t"
      "v_smfmac_f32_16x16x64_bf16 v[220:223], a[32:35], v[152:159], v224\n\t"
      "ds_read_b128 a[8:11], v225 offset:6144\n\t"
      "v_exp_f32 v133, v133\n\t"
      "v_exp_f32 v134, v134\n\t"
      "v_mfma_f32_32x32x16_bf16 v[144:159], a[0:3], v[184:187], v[80:95]\n\t"
      "v_exp_f32 v135, v135\n\t"
      "v_exp_f32 v136, v136\n\t"
      "v_exp_f32 v137, v137\n\t"
      "v_mfma_f32_32x32x16_bf16 v[144:159], a[4:7], v[188:191], v[144:159]\n\t"
      "s_waitcnt lgkmcnt(3)\n\t"
      "v_exp_f32 v138, v138\n\t"
      "v_exp_f32 v139, v139\n\t"
      "v_exp_f32 v140, v140\n\t"
      "v_smfmac_f32_32x32x32_bf16 v[0:15], a[16:19], v[128:135], v224\n\t"
      "s_nop 1\n\t"
      "v_exp_f32 v141, v141\n\t"
      "v_exp_f32 v142, v142\n\t"
      "v_exp_f32 v143, v143\n\t"
      "v_smfmac_f32_16x16x64_bf16 v[208:211], a[32:35], v[128:135], v224\n\t"
      "s_waitcnt lgkmcnt(1)\n\t"
      "ds_read_b128 a[12:15], v226 offset:6144\n\t"
      "v_exp_f32 v144, v144\n\t"
      "v_exp_f32 v145, v145\n\t"
      "v_smfmac_f32_32x32x32_bf16 v[0:15], a[24:27], v[136:143], v224\n\t"
      "v_exp_f32 v146, v146\n\t"
      "v_exp_f32 v147, v147\n\t"
      "v_exp_f32 v148, v148\n\t"
      "v_smfmac_f32_16x16x64_bf16 v[208:211], a[32:35], v[136:143], v224\n\t"
      "ds_read_b64_tr_b16 a[20:21], v227 offset:6144\n\t"
      "v_exp_f32 v149, v149\n\t"
      "v_exp_f32 v150, v150\n\t"
      "v_mfma_f32_32x32x16_bf16 v[128:143], a[0:3], v[192:195], v[96:111]\n\t"
      "v_exp_f32 v151, v151\n\t"
      "v_exp_f32 v152, v152\n\t"
      "v_exp_f32 v153, v153\n\t"
      "v_mfma_f32_32x32x16_bf16 v[128:143], a[4:7], v[196:199], v[128:143]\n\t"
      "s_nop 0\n\t"
      "v_exp_f32 v154, v154\n\t"
      "v_exp_f32 v155, v155\n\t"
      "v_exp_f32 v156, v156\n\t"
      "v_smfmac_f32_32x32x32_bf16 v[16:31], a[16:19], v[144:151], v224\n\t"
      "s_nop 1\n\t"
      "v_exp_f32 v157, v157\n\t"
      "v_exp_f32 v158, v158\n\t"
      "v_exp_f32 v159, v159\n\t"
      "v_smfmac_f32_16x16x64_bf16 v[212:215], a[32:35], v[144:151], v224\n\t"
      "ds_read_b64_tr_b16 a[22:23], v227 offset:6400\n\t"
      "v_exp_f32 v128, v128\n\t"
      "v_exp_f32 v129, v129\n\t"
      "v_smfmac_f32_32x32x32_bf16 v[16:31], a[24:27], v[152:159], v224\n\t"
      "v_exp_f32 v130, v130\n\t"
      "v_exp_f32 v131, v131\n\t"
      "v_exp_f32 v132, v132\n\t"
      "v_smfmac_f32_16x16x64_bf16 v[212:215], a[32:35], v[152:159], v224\n\t"
      "ds_read_b64_tr_b16 a[28:29], v227 offset:7168\n\t"
      "v_exp_f32 v133, v133\n\t"
      "v_exp_f32 v134, v134\n\t"
      "v_mfma_f32_32x32x16_bf16 v[144:159], a[0:3], v[200:203], v[112:127]\n\t"
      "v_exp_f32 v135, v135\n\t"
      "v_exp_f32 v136, v136\n\t"
      "v_exp_f32 v137, v137\n\t"
      "v_mfma_f32_32x32x16_bf16 v[144:159], a[4:7], v[204:207], v[144:159]\n\t"
      "s_nop 0\n\t"
      "v_exp_f32 v138, v138\n\t"
      "v_exp_f32 v139, v139\n\t"
      "v_exp_f32 v140, v140\n\t"
      "v_smfmac_f32_32x32x32_bf16 v[32:47], a[16:19], v[128:135], v224\n\t"
      "s_nop 1\n\t"
      "v_exp_f32 v141, v141\n\t"
      "v_exp_f32 v142, v142\n\t"
      "v_exp_f32 v143, v143\n\t"
      "v_smfmac_f32_16x16x64_bf16 v[216:219], a[32:35], v[128:135], v224\n\t"
      "ds_read_b64_tr_b16 a[30:31], v227 offset:7424\n\t"
      "v_exp_f32 v144, v144\n\t"
      "v_exp_f32 v145, v145\n\t"
      "v_smfmac_f32_32x32x32_bf16 v[32:47], a[24:27], v[136:143], v224\n\t"
      "v_exp_f32 v146, v146\n\t"
      "v_exp_f32 v147, v147\n\t"
      "v_exp_f32 v148, v148\n\t"
      "v_smfmac_f32_16x16x64_bf16 v[216:219], a[32:35], v[136:143], v224\n\t"
      "s_waitcnt lgkmcnt(4)\n\t"
      "v_exp_f32 v149, v149\n\t"
      "v_exp_f32 v150, v150\n\t"
      "v_mfma_f32_32x32x16_bf16 v[128:143], a[8:11], v[176:179], v[64:79]\n\t"
      "v_exp_f32 v151, v151\n\t"
      "v_exp_f32 v152, v152\n\t"
      "v_exp_f32 v153, v153\n\t"
      "v_mfma_f32_32x32x16_bf16 v[128:143], a[12:15], v[180:183], v[128:143]\n\t"
      "s_nop 0\n\t"
      "v_exp_f32 v154, v154\n\t"
      "v_exp_f32 v155, v155\n\t"
      "v_exp_f32 v156, v156\n\t"
      "v_smfmac_f32_32x32x32_bf16 v[48:63], a[16:19], v[144:151], v224\n\t"
      "s_nop 1\n\t"
      "v_exp_f32 v157, v157\n\t"
      "v_exp_f32 v158, v158\n\t"
      "v_exp_f32 v159, v159\n\t"
      "v_smfmac_f32_16x16x64_bf16 v[220:223], a[32:35], v[144:151], v224\n\t"
      "s_nop 0\n\t"
      "v_exp_f32 v128, v128\n\t"
      "v_exp_f32 v129, v129\n\t"
      "v_smfmac_f32_32x32x32_bf16 v[48:63], a[24:27], v[152:159], v224\n\t"
      "v_exp_f32 v130, v130\n\t"
      "v_exp_f32 v131, v131\n\t"
      "v_exp_f32 v132, v132\n\t"
      "v_smfmac_f32_16x16x64_bf16 v[220:223], a[32:35], v[152:159], v224\n\t"
      "ds_read_b128 a[0:3], v225 offset:8192\n\t"
      "v_exp_f32 v133, v133\n\t"
      "v_exp_f32 v134, v134\n\t"
      "v_mfma_f32_32x32x16_bf16 v[144:159], a[8:11], v[184:187], v[80:95]\n\t"
      "v_exp_f32 v135, v135\n\t"
      "v_exp_f32 v136, v136\n\t"
      "v_exp_f32 v137, v137\n\t"
      "v_mfma_f32_32x32x16_bf16 v[144:159], a[12:15], v[188:191], v[144:159]\n\t"
      "s_waitcnt lgkmcnt(3)\n\t"
      "v_exp_f32 v138, v138\n\t"
      "v_exp_f32 v139, v139\n\t"
      "v_exp_f32 v140, v140\n\t"
      "v_smfmac_f32_32x32x32_bf16 v[0:15], a[20:23], v[128:135], v224\n\t"
      "s_nop 1\n\t"
      "v_exp_f32 v141, v141\n\t"
      "v_exp_f32 v142, v142\n\t"
      "v_exp_f32 v143, v143\n\t"
      "v_smfmac_f32_16x16x64_bf16 v[208:211], a[32:35], v[128:135], v224\n\t"
      "s_waitcnt lgkmcnt(1)\n\t"
      "ds_read_b128 a[4:7], v226 offset:8192\n\t"
      "v_exp_f32 v144, v144\n\t"
      "v_exp_f32 v145, v145\n\t"
      "v_smfmac_f32_32x32x32_bf16 v[0:15], a[28:31], v[136:143], v224\n\t"
      "v_exp_f32 v146, v146\n\t"
      "v_exp_f32 v147, v147\n\t"
      "v_exp_f32 v148, v148\n\t"
      "v_smfmac_f32_16x16x64_bf16 v[208:211], a[32:35], v[136:143], v224\n\t"
      "ds_read_b64_tr_b16 a[16:17], v227 offset:8192\n\t"
      "v_exp_f32 v149, v149\n\t"
      "v_exp_f32 v150, v150\n\t"
      "v_mfma_f32_32x32x16_bf16 v[128:143], a[8:11], v[192:195], v[96:111]\n\t"
      "v_exp_f32 v151, v151\n\t"
      "v_exp_f32 v152, v152\n\t"
      "v_exp_f32 v153, v153\n\t"
      "v_mfma_f32_32x32x16_bf16 v[128:143], a[12:15], v[196:199], v[128:143]\n\t"
      "s_nop 0\n\t"
      "v_exp_f32 v154, v154\n\t"
      "v_exp_f32 v155, v155\n\t"
      "v_exp_f32 v156, v156\n\t"
      "v_smfmac_f32_32x32x32_bf16 v[16:31], a[20:23], v[144:151], v224\n\t"
      "s_nop 1\n\t"
      "v_exp_f32 v157, v157\n\t"
      "v_exp_f32 v158, v158\n\t"
      "v_exp_f32 v159, v159\n\t"
      "v_smfmac_f32_16x16x64_bf16 v[212:215], a[32:35], v[144:151], v224\n\t"
      "ds_read_b64_tr_b16 a[18:19], v227 offset:8448\n\t"
      "v_exp_f32 v128, v128\n\t"
      "v_exp_f32 v129, v129\n\t"
      "v_smfmac_f32_32x32x32_bf16 v[16:31], a[28:31], v[152:159], v224\n\t"
      "v_exp_f32 v130, v130\n\t"
      "v_exp_f32 v131, v131\n\t"
      "v_exp_f32 v132, v132\n\t"
      "v_smfmac_f32_16x16x64_bf16 v[212:215], a[32:35], v[152:159], v224\n\t"
      "ds_read_b64_tr_b16 a[24:25], v227 offset:9216\n\t"
      "v_exp_f32 v133, v133\n\t"
      "v_exp_f32 v134, v134\n\t"
      "v_mfma_f32_32x32x16_bf16 v[144:159], a[8:11], v[200:203], v[112:127]\n\t"
      "v_exp_f32 v135, v135\n\t"
      "v_exp_f32 v136, v136\n\t"
      "v_exp_f32 v137, v137\n\t"
      "v_mfma_f32_32x32x16_bf16 v[144:159], a[12:15], v[204:207], v[144:159]\n\t"
      "s_nop 0\n\t"
      "v_exp_f32 v138, v138\n\t"
      "v_exp_f32 v139, v139\n\t"
      "v_exp_f32 v140, v140\n\t"
      "v_smfmac_f32_32x32x32_bf16 v[32:47], a[20:23], v[128:135], v224\n\t"
      "s_nop 1\n\t"
      "v_exp_f32 v141, v141\n\t"
      "v_exp_f32 v142, v142\n\t"
      "v_exp_f32 v143, v143\n\t"
      "v_smfmac_f32_16x16x64_bf16 v[216:219], a[32:35], v[128:135], v224\n\t"
      "ds_read_b64_tr_b16 a[26:27], v227 offset:9472\n\t"
      "v_exp_f32 v144, v144\n\t"
      "v_exp_f32 v145, v145\n\t"
      "v_smfmac_f32_32x32x32_bf16 v[32:47], a[28:31], v[136:143], v224\n\t"
      "v_exp_f32 v146, v146\n\t"
      "v_exp_f32 v147, v147\n\t"
      "v_exp_f32 v148, v148\n\t"
      "v_smfmac_f32_16x16x64_bf16 v[216:219], a[32:35], v[136:143], v224\n\t"
      "s_waitcnt lgkmcnt(4)\n\t"
      "v_exp_f32 v149, v149\n\t"
      "v_exp_f32 v150, v150\n\t"
      "v_mfma_f32_32x32x16_bf16 v[128:143], a[0:3], v[176:179], v[64:79]\n\t"
      "v_exp_f32 v151, v151\n\t"
      "v_exp_f32 v152, v152\n\t"
      "v_exp_f32 v153, v153\n\t"
      "v_mfma_f32_32x32x16_bf16 v[128:143], a[4:7], v[180:183], v[128:143]\n\t"
      "s_nop 0\n\t"
      "v_exp_f32 v154, v154\n\t"
      "v_exp_f32 v155, v155\n\t"
      "v_exp_f32 v156, v156\n\t"
      "v_smfmac_f32_32x32x32_bf16 v[48:63], a[20:23], v[144:151], v224\n\t"
      "s_nop 1\n\t"
      "v_exp_f32 v157, v157\n\t"
      "v_exp_f32 v158, v158\n\t"
      "v_exp_f32 v159, v159\n\t"
      "v_smfmac_f32_16x16x64_bf16 v[220:223], a[32:35], v[144:151], v224\n\t"
      "s_nop 0\n\t"
      "v_exp_f32 v128, v128\n\t"
      "v_exp_f32 v129, v129\n\t"
      "v_smfmac_f32_32x32x32_bf16 v[48:63], a[28:31], v[152:159], v224\n\t"
      "v_exp_f32 v130, v130\n\t"
      "v_exp_f32 v131, v131\n\t"
      "v_exp_f32 v132, v132\n\t"
      "v_smfmac_f32_16x16x64_bf16 v[220:223], a[32:35], v[152:159], v224\n\t"
      "ds_read_b128 a[8:11], v225 offset:10240\n\t"
      "v_exp_f32 v133, v133\n\t"
      "v_exp_f32 v134, v134\n\t"
      "v_mfma_f32_32x32x16_bf16 v[144:159], a[0:3], v[184:187], v[80:95]\n\t"
      "v_exp_f32 v135, v135\n\t"
      "v_exp_f32 v136, v136\n\t"
      "v_exp_f32 v137, v137\n\t"
      "v_mfma_f32_32x32x16_bf16 v[144:159], a[4:7], v[188:191], v[144:159]\n\t"
      "s_waitcnt lgkmcnt(3)\n\t"
      "v_exp_f32 v138, v138\n\t"
      "v_exp_f32 v139, v139\n\t"
      "v_exp_f32 v140, v140\n\t"
      "v_smfmac_f32_32x32x32_bf16 v[0:15], a[16:19], v[128:135], v224\n\t"
      "s_nop 1\n\t"
      "v_exp_f32 v141, v141\n\t"
      "v_exp_f32 v142, v142\n\t"
      "v_exp_f32 v143, v143\n\t"
      "v_smfmac_f32_16x16x64_bf16 v[208:211], a[32:35], v[128:135], v224\n\t"
      "s_waitcnt lgkmcnt(1)\n\t"
      "ds_read_b128 a[12:15], v226 offset:10240\n\t"
      "v_exp_f32 v144, v144\n\t"
      "v_exp_f32 v145, v145\n\t"
      "v_smfmac_f32_32x32x32_bf16 v[0:15], a[24:27], v[136:143], v224\n\t"
      "v_exp_f32 v146, v146\n\t"
      "v_exp_f32 v147, v147\n\t"
      "v_exp_f32 v148, v148\n\t"
      "v_smfmac_f32_16x16x64_bf16 v[208:211], a[32:35], v[136:143], v224\n\t"
      "ds_read_b64_tr_b16 a[20:21], v227 offset:10240\n\t"
      "v_exp_f32 v149, v149\n\t"
      "v_exp_f32 v150, v150\n\t"
      "v_mfma_f32_32x32x16_bf16 v[128:143], a[0:3], v[192:195], v[96:111]\n\t"
      "v_exp_f32 v151, v151\n\t"
      "v_exp_f32 v152, v152\n\t"
      "v_exp_f32 v153, v153\n\t"
      "v_mfma_f32_32x32x16_bf16 v[128:143], a[4:7], v[196:199], v[128:143]\n\t"
      "s_nop 0\n\t"
      "v_exp_f32 v154, v154\n\t"
      "v_exp_f32 v155, v155\n\t"
      "v_exp_f32 v156, v156\n\t"
      "v_smfmac_f32_32x32x32_bf16 v[16:31], a[16:19], v[144:151], v224\n\t"
      "s_nop 1\n\t"
      "v_exp_f32 v157, v157\n\t"
      "v_exp_f32 v158, v158\n\t"
      "v_exp_f32 v159, v159\n\t"
      "v_smfmac_f32_16x16x64_bf16 v[212:215], a[32:35], v[144:151], v224\n\t"
      "ds_read_b64_tr_b16 a[22:23], v227 offset:10496\n\t"
      "v_exp_f32 v128, v128\n\t"
      "v_exp_f32 v129, v129\n\t"
      "v_smfmac_f32_32x32x32_bf16 v[16:31], a[24:27], v[152:159], v224\n\t"
      "v_exp_f32 v130, v130\n\t"
      "v_exp_f32 v131, v131\n\t"
      "v_exp_f32 v132, v132\n\t"
      "v_smfmac_f32_16x16x64_bf16 v[212:215], a[32:35], v[152:159], v224\n\t"
      "ds_read_b64_tr_b16 a[28:29], v227 offset:11264\n\t"
      "v_exp_f32 v133, v133\n\t"
      "v_exp_f32 v134, v134\n\t"
      "v_mfma_f32_32x32x16_bf16 v[144:159], a[0:3], v[200:203], v[112:127]\n\t"
      "v_exp_f32 v135, v135\n\t"
      "v_exp_f32 v136, v136\n\t"
      "v_exp_f32 v137, v137\n\t"
      "v_mfma_f32_32x32x16_bf16 v[144:159], a[4:7], v[204:207], v[144:159]\n\t"
      "s_nop 0\n\t"
      "v_exp_f32 v138, v138\n\t"
      "v_exp_f32 v139, v139\n\t"
      "v_exp_f32 v140, v140\n\t"
      "v_smfmac_f32_32x32x32_bf16 v[32:47], a[16:19], v[128:135], v224\n\t"
      "s_nop 1\n\t"
      "v_exp_f32 v141, v141\n\t"
      "v_exp_f32 v142, v142\n\t"
      "v_exp_f32 v143, v143\n\t"
      "v_smfmac_f32_16x16x64_bf16 v[216:219], a[32:35], v[128:135], v224\n\t"
      "ds_read_b64_tr_b16 a[30:31], v227 offset:11520\n\t"
      "v_exp_f32 v144, v144\n\t"
      "v_exp_f32 v145, v145\n\t"
      "v_smfmac_f32_32x32x32_bf16 v[32:47], a[24:27], v[136:143], v224\n\t"
      "v_exp_f32 v146, v146\n\t"
      "v_exp_f32 v147, v147\n\t"
      "v_exp_f32 v148, v148\n\t"
      "v_smfmac_f32_16x16x64_bf16 v[216:219], a[32:35], v[136:143], v224\n\t"
      "s_waitcnt lgkmcnt(4)\n\t"
      "v_exp_f32 v149, v149\n\t"
      "v_exp_f32 v150, v150\n\t"
      "v_mfma_f32_32x32x16_bf16 v[128:143], a[8:11], v[176:179], v[64:79]\n\t"
      "v_exp_f32 v151, v151\n\t"
      "v_exp_f32 v152, v152\n\t"
      "v_exp_f32 v153, v153\n\t"
      "v_mfma_f32_32x32x16_bf16 v[128:143], a[12:15], v[180:183], v[128:143]\n\t"
      "s_nop 0\n\t"
      "v_exp_f32 v154, v154\n\t"
      "v_exp_f32 v155, v155\n\t"
      "v_exp_f32 v156, v156\n\t"
      "v_smfmac_f32_32x32x32_bf16 v[48:63], a[16:19], v[144:151], v224\n\t"
      "s_nop 1\n\t"
      "v_exp_f32 v157, v157\n\t"
      "v_exp_f32 v158, v158\n\t"
      "v_exp_f32 v159, v159\n\t"
      "v_smfmac_f32_16x16x64_bf16 v[220:223], a[32:35], v[144:151], v224\n\t"
      "s_nop 0\n\t"
      "v_exp_f32 v128, v128\n\t"
      "v_exp_f32 v129, v129\n\t"
      "v_smfmac_f32_32x32x32_bf16 v[48:63], a[24:27], v[152:159], v224\n\t"
      "v_exp_f32 v130, v130\n\t"
      "v_exp_f32 v131, v131\n\t"
      "v_exp_f32 v132, v132\n\t"
      "v_smfmac_f32_16x16x64_bf16 v[220:223], a[32:35], v[152:159], v224\n\t"
      "ds_read_b128 a[0:3], v225 offset:12288\n\t"
      "v_exp_f32 v133, v133\n\t"
      "v_exp_f32 v134, v134\n\t"
      "v_mfma_f32_32x32x16_bf16 v[144:159], a[8:11], v[184:187], v[80:95]\n\t"
      "v_exp_f32 v135, v135\n\t"
      "v_exp_f32 v136, v136\n\t"
      "v_exp_f32 v137, v137\n\t"
      "v_mfma_f32_32x32x16_bf16 v[144:159], a[12:15], v[188:191], v[144:159]\n\t"
      "s_waitcnt lgkmcnt(3)\n\t"
      "v_exp_f32 v138, v138\n\t"
      "v_exp_f32 v139, v139\n\t"
      "v_exp_f32 v140, v140\n\t"
      "v_smfmac_f32_32x32x32_bf16 v[0:15], a[20:23], v[128:135], v224\n\t"
      "s_nop 1\n\t"
      "v_exp_f32 v141, v141\n\t"
      "v_exp_f32 v142, v142\n\t"
      "v_exp_f32 v143, v143\n\t"
      "v_smfmac_f32_16x16x64_bf16 v[208:211], a[32:35], v[128:135], v224\n\t"
      "s_waitcnt lgkmcnt(1)\n\t"
      "ds_read_b128 a[4:7], v226 offset:12288\n\t"
      "v_exp_f32 v144, v144\n\t"
      "v_exp_f32 v145, v145\n\t"
      "v_smfmac_f32_32x32x32_bf16 v[0:15], a[28:31], v[136:143], v224\n\t"
      "v_exp_f32 v146, v146\n\t"
      "v_exp_f32 v147, v147\n\t"
      "v_exp_f32 v148, v148\n\t"
      "v_smfmac_f32_16x16x64_bf16 v[208:211], a[32:35], v[136:143], v224\n\t"
      "ds_read_b64_tr_b16 a[16:17], v227 offset:12288\n\t"
      "v_exp_f32 v149, v149\n\t"
      "v_exp_f32 v150, v150\n\t"
      "v_mfma_f32_32x32x16_bf16 v[128:143], a[8:11], v[192:195], v[96:111]\n\t"
      "v_exp_f32 v151, v151\n\t"
      "v_exp_f32 v152, v152\n\t"
      "v_exp_f32 v153, v153\n\t"
      "v_mfma_f32_32x32x16_bf16 v[128:143], a[12:15], v[196:199], v[128:143]\n\t"
      "s_nop 0\n\t"
      "v_exp_f32 v154, v154\n\t"
      "v_exp_f32 v155, v155\n\t"
      "v_exp_f32 v156, v156\n\t"
      "v_smfmac_f32_32x32x32_bf16 v[16:31], a[20:23], v[144:151], v224\n\t"
      "s_nop 1\n\t"
      "v_exp_f32 v157, v157\n\t"
      "v_exp_f32 v158, v158\n\t"
      "v_exp_f32 v159, v159\n\t"
      "v_smfmac_f32_16x16x64_bf16 v[212:215], a[32:35], v[144:151], v224\n\t"
      "ds_read_b64_tr_b16 a[18:19], v227 offset:12544\n\t"
      "v_exp_f32 v128, v128\n\t"
      "v_exp_f32 v129, v129\n\t"
      "v_smfmac_f32_32x32x32_bf16 v[16:31], a[28:31], v[152:159], v224\n\t"
      "v_exp_f32 v130, v130\n\t"
      "v_exp_f32 v131, v131\n\t"
      "v_exp_f32 v132, v132\n\t"
      "v_smfmac_f32_16x16x64_bf16 v[212:215], a[32:35], v[152:159], v224\n\t"
      "ds_read_b64_tr_b16 a[24:25], v227 offset:13312\n\t"
      "v_exp_f32 v133, v133\n\t"
      "v_exp_f32 v134, v134\n\t"
      "v_mfma_f32_32x32x16_bf16 v[144:159], a[8:11], v[200:203], v[112:127]\n\t"
      "v_exp_f32 v135, v135\n\t"
      "v_exp_f32 v136, v136\n\t"
      "v_exp_f32 v137, v137\n\t"
      "v_mfma_f32_32x32x16_bf16 v[144:159], a[12:15], v[204:207], v[144:159]\n\t"
      "s_nop 0\n\t"
      "v_exp_f32 v138, v138\n\t"
      "v_exp_f32 v139, v139\n\t"
      "v_exp_f32 v140, v140\n\t"
      "v_smfmac_f32_32x32x32_bf16 v[32:47], a[20:23], v[128:135], v224\n\t"
      "s_nop 1\n\t"
      "v_exp_f32 v141, v141\n\t"
      "v_exp_f32 v142, v142\n\t"
      "v_exp_f32 v143, v143\n\t"
      "v_smfmac_f32_16x16x64_bf16 v[216:219], a[32:35], v[128:135], v224\n\t"
      "ds_read_b64_tr_b16 a[26:27], v227 offset:13568\n\t"
      "v_exp_f32 v144, v144\n\t"
      "v_exp_f32 v145, v145\n\t"
      "v_smfmac_f32_32x32x32_bf16 v[32:47], a[28:31], v[136:143], v224\n\t"
      "v_exp_f32 v146, v146\n\t"
      "v_exp_f32 v147, v147\n\t"
      "v_exp_f32 v148, v148\n\t"
      "v_smfmac_f32_16x16x64_bf16 v[216:219], a[32:35], v[136:143], v224\n\t"
      "s_waitcnt lgkmcnt(4)\n\t"
      "v_exp_f32 v149, v149\n\t"
      "v_exp_f32 v150, v150\n\t"
      "v_mfma_f32_32x32x16_bf16 v[128:143], a[0:3], v[176:179], v[64:79]\n\t"
      "v_exp_f32 v151, v151\n\t"
      "v_exp_f32 v152, v152\n\t"
      "v_exp_f32 v153, v153\n\t"
      "v_mfma_f32_32x32x16_bf16 v[128:143], a[4:7], v[180:183], v[128:143]\n\t"
      "s_nop 0\n\t"
      "v_exp_f32 v154, v154\n\t"
      "v_exp_f32 v155, v155\n\t"
      "v_exp_f32 v156, v156\n\t"
      "v_smfmac_f32_32x32x32_bf16 v[48:63], a[20:23], v[144:151], v224\n\t"
      "s_nop 1\n\t"
      "v_exp_f32 v157, v157\n\t"
      "v_exp_f32 v158, v158\n\t"
      "v_exp_f32 v159, v159\n\t"
      "v_smfmac_f32_16x16x64_bf16 v[220:223], a[32:35], v[144:151], v224\n\t"
      "s_nop 0\n\t"
      "v_exp_f32 v128, v128\n\t"
      "v_exp_f32 v129, v129\n\t"
      "v_smfmac_f32_32x32x32_bf16 v[48:63], a[28:31], v[152:159], v224\n\t"
      "v_exp_f32 v130, v130\n\t"
      "v_exp_f32 v131, v131\n\t"
      "v_exp_f32 v132, v132\n\t"
      "v_smfmac_f32_16x16x64_bf16 v[220:223], a[32:35], v[152:159], v224\n\t"
      "ds_read_b128 a[8:11], v225 offset:14336\n\t"
      "v_exp_f32 v133, v133\n\t"
      "v_exp_f32 v134, v134\n\t"
      "v_mfma_f32_32x32x16_bf16 v[144:159], a[0:3], v[184:187], v[80:95]\n\t"
      "v_exp_f32 v135, v135\n\t"
      "v_exp_f32 v136, v136\n\t"
      "v_exp_f32 v137, v137\n\t"
      "v_mfma_f32_32x32x16_bf16 v[144:159], a[4:7], v[188:191], v[144:159]\n\t"
      "s_waitcnt lgkmcnt(3)\n\t"
      "v_exp_f32 v138, v138\n\t"
      "v_exp_f32 v139, v139\n\t"
      "v_exp_f32 v140, v140\n\t"
      "v_smfmac_f32_32x32x32_bf16 v[0:15], a[16:19], v[128:135], v224\n\t"
      "s_nop 1\n\t"
      "v_exp_f32 v141, v141\n\t"
      "v_exp_f32 v142, v142\n\t"
      "v_exp_f32 v143, v143\n\t"
      "v_smfmac_f32_16x16x64_bf16 v[208:211], a[32:35], v[128:135], v224\n\t"
      "s_waitcnt lgkmcnt(1)\n\t"
      "ds_read_b128 a[12:15], v226 offset:14336\n\t"
      "v_exp_f32 v144, v144\n\t"
      "v_exp_f32 v145, v145\n\t"
      "v_smfmac_f32_32x32x32_bf16 v[0:15], a[24:27], v[136:143], v224\n\t"
      "v_exp_f32 v146, v146\n\t"
      "v_exp_f32 v147, v147\n\t"
      "v_exp_f32 v148, v148\n\t"
      "v_smfmac_f32_16x16x64_bf16 v[208:211], a[32:35], v[136:143], v224\n\t"
      "ds_read_b64_tr_b16 a[20:21], v227 offset:14336\n\t"
      "v_exp_f32 v149, v149\n\t"
      "v_exp_f32 v150, v150\n\t"
      "v_mfma_f32_32x32x16_bf16 v[128:143], a[0:3], v[192:195], v[96:111]\n\t"
      "v_exp_f32 v151, v151\n\t"
      "v_exp_f32 v152, v152\n\t"
      "v_exp_f32 v153, v153\n\t"
      "v_mfma_f32_32x32x16_bf16 v[128:143], a[4:7], v[196:199], v[128:143]\n\t"
      "s_nop 0\n\t"
      "v_exp_f32 v154, v154\n\t"
      "v_exp_f32 v155, v155\n\t"
      "v_exp_f32 v156, v156\n\t"
      "v_smfmac_f32_32x32x32_bf16 v[16:31], a[16:19], v[144:151], v224\n\t"
      "s_nop 1\n\t"
      "v_exp_f32 v157, v157\n\t"
      "v_exp_f32 v158, v158\n\t"
      "v_exp_f32 v159, v159\n\t"
      "v_smfmac_f32_16x16x64_bf16 v[212:215], a[32:35], v[144:151], v224\n\t"
      "ds_read_b64_tr_b16 a[22:23], v227 offset:14592\n\t"
      "v_exp_f32 v128, v128\n\t"
      "v_exp_f32 v129, v129\n\t"
      "v_smfmac_f32_32x32x32_bf16 v[16:31], a[24:27], v[152:159], v224\n\t"
      "s_add_u32 s64, s64, 1\n\t"
      "s_and_b32 s65, s64, 3\n\t"
      "v_exp_f32 v130, v130\n\t"
      "v_exp_f32 v131, v131\n\t"
      "v_exp_f32 v132, v132\n\t"
      "v_smfmac_f32_16x16x64_bf16 v[212:215], a[32:35], v[152:159], v224\n\t"
      "ds_read_b64_tr_b16 a[28:29], v227 offset:15360\n\t"
      "v_exp_f32 v133, v133\n\t"
      "v_exp_f32 v134, v134\n\t"
      "v_mfma_f32_32x32x16_bf16 v[144:159], a[0:3], v[200:203], v[112:127]\n\t"
      "v_exp_f32 v135, v135\n\t"
      "v_exp_f32 v136, v136\n\t"
      "v_exp_f32 v137, v137\n\t"
      "v_mfma_f32_32x32x16_bf16 v[144:159], a[4:7], v[204:207], v[144:159]\n\t"
      "s_nop 0\n\t"
      "v_exp_f32 v138, v138\n\t"
      "v_exp_f32 v139, v139\n\t"
      "v_exp_f32 v140, v140\n\t"
      "v_smfmac_f32_32x32x32_bf16 v[32:47], a[16:19], v[128:135], v224\n\t"
      "s_nop 1\n\t"
      "v_exp_f32 v141, v141\n\t"
      "v_exp_f32 v142, v142\n\t"
      "v_exp_f32 v143, v143\n\t"
      "v_smfmac_f32_16x16x64_bf16 v[216:219], a[32:35], v[128:135], v224\n\t"
      "ds_read_b64_tr_b16 a[30:31], v227 offset:15616\n\t"
      "v_exp_f32 v144, v144\n\t"
      "v_exp_f32 v145, v145\n\t"
      "v_smfmac_f32_32x32x32_bf16 v[32:47], a[24:27], v[136:143], v224\n\t"
      "s_lshl_b32 s65, s65, 15\n\t"
      "v_exp_f32 v146, v146\n\t"
      "v_exp_f32 v147, v147\n\t"
      "v_exp_f32 v148, v148\n\t"
      "v_smfmac_f32_16x16x64_bf16 v[216:219], a[32:35], v[136:143], v224\n\t"
      "s_waitcnt lgkmcnt(4)\n\t"
      "v_exp_f32 v149, v149\n\t"
      "v_exp_f32 v150, v150\n\t"
      "v_mfma_f32_32x32x16_bf16 v[128:143], a[8:11], v[176:179], v[64:79]\n\t"
      "v_exp_f32 v151, v151\n\t"
      "v_exp_f32 v152, v152\n\t"
      "v_exp_f32 v153, v153\n\t"
      "v_mfma_f32_32x32x16_bf16 v[128:143], a[12:15], v[180:183], v[128:143]\n\t"
      "s_nop 0\n\t"
      "v_exp_f32 v154, v154\n\t"
      "v_exp_f32 v155, v155\n\t"
      "v_exp_f32 v156, v156\n\t"
      "v_smfmac_f32_32x32x32_bf16 v[48:63], a[16:19], v[144:151], v224\n\t"
      "s_waitcnt vmcnt(0)\n\t"
      "s_barrier\n\t"
      "v_exp_f32 v157, v157\n\t"
      "v_exp_f32 v158, v158\n\t"
      "v_exp_f32 v159, v159\n\t"
      "v_smfmac_f32_16x16x64_bf16 v[220:223], a[32:35], v[144:151], v224\n\t"
      "s_nop 0\n\t"
      "v_add_u32 v225, s65, v230\n\t"
      "v_add_u32 v226, s65, v231\n\t"
      "v_add_u32 v227, s65, v232\n\t"
      "v_exp_f32 v128, v128\n\t"
      "v_exp_f32 v129, v129\n\t"
      "v_smfmac_f32_32x32x32_bf16 v[48:63], a[24:27], v[152:159], v224\n\t"
      "v_exp_f32 v130, v130\n\t"
      "v_exp_f32 v131, v131\n\t"
      "v_exp_f32 v132, v132\n\t"
      "v_smfmac_f32_16x16x64_bf16 v[220:223], a[32:35], v[152:159], v224\n\t"
      "ds_read_b128 a[0:3], v225 offset:0\n\t"
      "v_exp_f32 v133, v133\n\t"
      "v_exp_f32 v134, v134\n\t"
      "v_mfma_f32_32x32x16_bf16 v[144:159], a[8:11], v[184:187], v[80:95]\n\t"
      "v_exp_f32 v135, v135\n\t"
      "v_exp_f32 v136, v136\n\t"
      "v_exp_f32 v137, v137\n\t"
      "v_mfma_f32_32x32x16_bf16 v[144:159], a[12:15], v[188:191], v[144:159]\n\t"
      "s_waitcnt lgkmcnt(3)\n\t"
      "v_exp_f32 v138, v138\n\t"
      "v_exp_f32 v139, v139\n\t"
      "v_exp_f32 v140, v140\n\t"
      "v_smfmac_f32_32x32x32_bf16 v[0:15], a[20:23], v[128:135], v224\n\t"
      "s_nop 1\n\t"
      "v_exp_f32 v141, v141\n\t"
      "v_exp_f32 v142, v142\n\t"
      "v_exp_f32 v143, v143\n\t"
      "v_smfmac_f32_16x16x64_bf16 v[208:211], a[32:35], v[128:135], v224\n\t"
      "s_waitcnt lgkmcnt(1)\n\t"
      "ds_read_b128 a[4:7], v226 offset:0\n\t"
      "v_exp_f32 v144, v144\n\t"
      "v_exp_f32 v145, v145\n\t"
      "v_smfmac_f32_32x32x32_bf16 v[0:15], a[28:31], v[136:143], v224\n\t"
      "v_exp_f32 v146, v146\n\t"
      "v_exp_f32 v147, v147\n\t"
      "v_exp_f32 v148, v148\n\t"
      "v_smfmac_f32_16x16x64_bf16 v[208:211], a[32:35], v[136:143], v224\n\t"
      "ds_read_b64_tr_b16 a[16:17], v227 offset:0\n\t"
      "v_exp_f32 v149, v149\n\t"
      "v_exp_f32 v150, v150\n\t"
      "v_mfma_f32_32x32x16_bf16 v[128:143], a[8:11], v[192:195], v[96:111]\n\t"
      "v_exp_f32 v151, v151\n\t"
      "v_exp_f32 v152, v152\n\t"
      "v_exp_f32 v153, v153\n\t"
      "v_mfma_f32_32x32x16_bf16 v[128:143], a[12:15], v[196:199], v[128:143]\n\t"
      "s_nop 0\n\t"
      "v_exp_f32 v154, v154\n\t"
      "v_exp_f32 v155, v155\n\t"
      "v_exp_f32 v156, v156\n\t"
      "v_smfmac_f32_32x32x32_bf16 v[16:31], a[20:23], v[144:151], v224\n\t"
      "s_nop 1\n\t"
      "v_exp_f32 v157, v157\n\t"
      "v_exp_f32 v158, v158\n\t"
      "v_exp_f32 v159, v159\n\t"
      "v_smfmac_f32_16x16x64_bf16 v[212:215], a[32:35], v[144:151], v224\n\t"
      "ds_read_b64_tr_b16 a[18:19], v227 offset:256\n\t"
      "v_mov_b32 v209, v208\n\t"
      "s_nop 1\n\t"
      "v_permlane32_swap_b32 v208, v209\n\t"
      "v_rcp_f32 v164, v208\n\t"
      "s_nop 0\n\t"
      "v_pk_mul_f32 v[0:1], v[0:1], v[164:165] op_sel_hi:[1,0]\n\t"
      "v_pk_mul_f32 v[2:3], v[2:3], v[164:165] op_sel_hi:[1,0]\n\t"
      "v_pk_mul_f32 v[4:5], v[4:5], v[164:165] op_sel_hi:[1,0]\n\t"
      "v_pk_mul_f32 v[6:7], v[6:7], v[164:165] op_sel_hi:[1,0]\n\t"
      "v_pk_mul_f32 v[8:9], v[8:9], v[164:165] op_sel_hi:[1,0]\n\t"
      "v_pk_mul_f32 v[10:11], v[10:11], v[164:165] op_sel_hi:[1,0]\n\t"
      "v_pk_mul_f32 v[12:13], v[12:13], v[164:165] op_sel_hi:[1,0]\n\t"
      "v_pk_mul_f32 v[14:15], v[14:15], v[164:165] op_sel_hi:[1,0]\n\t"
      "v_cvt_pk_bf16_f32 v64, v0, v1\n\t"
      "v_cvt_pk_bf16_f32 v65, v2, v3\n\t"
      "v_cvt_pk_bf16_f32 v66, v4, v5\n\t"
      "v_cvt_pk_bf16_f32 v67, v6, v7\n\t"
      "v_cvt_pk_bf16_f32 v68, v8, v9\n\t"
      "v_cvt_pk_bf16_f32 v69, v10, v11\n\t"
      "v_cvt_pk_bf16_f32 v70, v12, v13\n\t"
      "v_cvt_pk_bf16_f32 v71, v14, v15\n\t"
      "ds_write_b64 v240, v[64:65] offset:0\n\t"
      "ds_write_b64 v240, v[66:67] offset:16\n\t"
      "ds_write_b64 v240, v[68:69] offset:32\n\t"
      "ds_write_b64 v240, v[70:71] offset:48\n\t"
      "ds_read_b128 v[72:75], v241 offset:0\n\t"
      "ds_read_b128 v[76:79], v241 offset:1280\n\t"
      "v_exp_f32 v128, v128\n\t"
      "v_exp_f32 v129, v129\n\t"
      "v_smfmac_f32_32x32x32_bf16 v[16:31], a[28:31], v[152:159], v224\n\t"
      "v_exp_f32 v130, v130\n\t"
      "v_exp_f32 v131, v131\n\t"
      "v_exp_f32 v132, v132\n\t"
      "v_smfmac_f32_16x16x64_bf16 v[212:215], a[32:35], v[152:159], v224\n\t"
      "v_mfma_f32_16x16x32_bf16 v[160:163], v[64:67], v[64:67], 0\n\t"
      "ds_read_b64_tr_b16 a[24:25], v227 offset:1024\n\t"
      "v_exp_f32 v133, v133\n\t"
      "v_exp_f32 v134, v134\n\t"
      "v_mfma_f32_32x32x16_bf16 v[144:159], a[8:11], v[200:203], v[112:127]\n\t"
      "s_waitcnt lgkmcnt(1)\n\t"
      "v_exp_f32 v135, v135\n\t"
      "v_exp_f32 v136, v136\n\t"
      "v_exp_f32 v137, v137\n\t"
      "v_mfma_f32_32x32x16_bf16 v[144:159], a[12:15], v[204:207], v[144:159]\n\t"
      "global_store_dwordx4 v239, v[72:75], %[optr] offset:0 nt\n\t"
      "v_exp_f32 v138, v138\n\t"
      "v_exp_f32 v139, v139\n\t"
      "v_exp_f32 v140, v140\n\t"
      "v_smfmac_f32_32x32x32_bf16 v[32:47], a[20:23], v[128:135], v224\n\t"
      "s_nop 0\n\t"
      "global_store_dwordx4 v239, v[76:79], %[optr] offset:1024 nt\n\t"
      "v_exp_f32 v141, v141\n\t"
      "v_exp_f32 v142, v142\n\t"
      "v_exp_f32 v143, v143\n\t"
      "v_smfmac_f32_16x16x64_bf16 v[216:219], a[32:35], v[128:135], v224\n\t"
      "v_mfma_f32_16x16x32_bf16 v[160:163], v[68:71], v[68:71], v[160:163]\n\t"
      "ds_read_b64_tr_b16 a[26:27], v227 offset:1280\n\t"
      "v_exp_f32 v144, v144\n\t"
      "v_exp_f32 v145, v145\n\t"
      "v_smfmac_f32_32x32x32_bf16 v[32:47], a[28:31], v[136:143], v224\n\t"
      "v_exp_f32 v146, v146\n\t"
      "v_exp_f32 v147, v147\n\t"
      "v_exp_f32 v148, v148\n\t"
      "v_smfmac_f32_16x16x64_bf16 v[216:219], a[32:35], v[136:143], v224\n\t"
      "s_waitcnt lgkmcnt(10)\n\t"
      "v_exp_f32 v149, v149\n\t"
      "v_exp_f32 v150, v150\n\t"
      "v_mfma_f32_32x32x16_bf16 v[128:143], a[0:3], v[184:187], v[80:95]\n\t"
      "v_exp_f32 v151, v151\n\t"
      "v_exp_f32 v152, v152\n\t"
      "v_exp_f32 v153, v153\n\t"
      "v_mfma_f32_32x32x16_bf16 v[128:143], a[4:7], v[188:191], v[128:143]\n\t"
      "s_nop 0\n\t"
      "v_exp_f32 v154, v154\n\t"
      "v_exp_f32 v155, v155\n\t"
      "v_exp_f32 v156, v156\n\t"
      "v_smfmac_f32_32x32x32_bf16 v[48:63], a[20:23], v[144:151], v224\n\t"
      "s_nop 1\n\t"
      "v_exp_f32 v157, v157\n\t"
      "v_exp_f32 v158, v158\n\t"
      "v_exp_f32 v159, v159\n\t"
      "v_smfmac_f32_16x16x64_bf16 v[220:223], a[32:35], v[144:151], v224\n\t"
      "s_nop 0\n\t"
      "v_exp_f32 v128, v128\n\t"
      "v_exp_f32 v129, v129\n\t"
      "v_smfmac_f32_32x32x32_bf16 v[48:63], a[28:31], v[152:159], v224\n\t"
      "v_exp_f32 v130, v130\n\t"
      "v_exp_f32 v131, v131\n\t"
      "v_exp_f32 v132, v132\n\t"
      "v_smfmac_f32_16x16x64_bf16 v[220:223], a[32:35], v[152:159], v224\n\t"
      "ds_read_b128 a[8:11], v225 offset:2048\n\t"
      "v_exp_f32 v133, v133\n\t"
      "v_exp_f32 v134, v134\n\t"
      "v_mfma_f32_32x32x16_bf16 v[144:159], a[0:3], v[192:195], v[96:111]\n\t"
      "v_exp_f32 v135, v135\n\t"
      "v_exp_f32 v136, v136\n\t"
      "v_exp_f32 v137, v137\n\t"
      "v_mfma_f32_32x32x16_bf16 v[144:159], a[4:7], v[196:199], v[144:159]\n\t"
      "s_waitcnt lgkmcnt(9)\n\t"
      "v_exp_f32 v138, v138\n\t"
      "v_exp_f32 v139, v139\n\t"
      "v_exp_f32 v140, v140\n\t"
      "v_smfmac_f32_32x32x32_bf16 v[16:31], a[16:19], v[128:135], v224\n\t"
      "s_nop 1\n\t"
      "v_exp_f32 v141, v141\n\t"
      "v_exp_f32 v142, v142\n\t"
      "v_exp_f32 v143, v143\n\t"
      "v_smfmac_f32_16x16x64_bf16 v[212:215], a[32:35], v[128:135], v224\n\t"
      "s_waitcnt lgkmcnt(1)\n\t"
      "ds_read_b128 a[12:15], v226 offset:2048\n\t"
      "v_exp_f32 v144, v144\n\t"
      "v_exp_f32 v145, v145\n\t"
      "v_smfmac_f32_32x32x32_bf16 v[16:31], a[24:27], v[136:143], v224\n\t"
      "v_exp_f32 v146, v146\n\t"
      "v_exp_f32 v147, v147\n\t"
      "v_exp_f32 v148, v148\n\t"
      "v_smfmac_f32_16x16x64_bf16 v[212:215], a[32:35], v[136:143], v224\n\t"
      "ds_read_b64_tr_b16 a[20:21], v227 offset:2048\n\t"
      "v_exp_f32 v149, v149\n\t"
      "v_exp_f32 v150, v150\n\t"
      "v_mfma_f32_32x32x16_bf16 v[128:143], a[0:3], v[200:203], v[112:127]\n\t"
      "v_exp_f32 v151, v151\n\t"
      "v_exp_f32 v152, v152\n\t"
      "v_exp_f32 v153, v153\n\t"
      "v_mfma_f32_32x32x16_bf16 v[128:143], a[4:7], v[204:207], v[128:143]\n\t"
      "s_nop 0\n\t"
      "v_exp_f32 v154, v154\n\t"
      "v_exp_f32 v155, v155\n\t"
      "v_exp_f32 v156, v156\n\t"
      "v_smfmac_f32_32x32x32_bf16 v[32:47], a[16:19], v[144:151], v224\n\t"
      "s_nop 1\n\t"
      "v_exp_f32 v157, v157\n\t"
      "v_exp_f32 v158, v158\n\t"
      "v_exp_f32 v159, v159\n\t"
      "v_smfmac_f32_16x16x64_bf16 v[216:219], a[32:35], v[144:151], v224\n\t"
      "ds_read_b64_tr_b16 a[22:23], v227 offset:2304\n\t"
      "v_mov_b32 v213, v212\n\t"
      "s_nop 1\n\t"
      "v_permlane32_swap_b32 v212, v213\n\t"
      "v_rcp_f32 v166, v212\n\t"
      "s_nop 0\n\t"
      "v_pk_mul_f32 v[16:17], v[16:17], v[166:167] op_sel_hi:[1,0]\n\t"
      "v_pk_mul_f32 v[18:19], v[18:19], v[166:167] op_sel_hi:[1,0]\n\t"
      "v_pk_mul_f32 v[20:21], v[20:21], v[166:167] op_sel_hi:[1,0]\n\t"
      "v_pk_mul_f32 v[22:23], v[22:23], v[166:167] op_sel_hi:[1,0]\n\t"
      "v_pk_mul_f32 v[24:25], v[24:25], v[166:167] op_sel_hi:[1,0]\n\t"
      "v_pk_mul_f32 v[26:27], v[26:27], v[166:167] op_sel_hi:[1,0]\n\t"
      "v_pk_mul_f32 v[28:29], v[28:29], v[166:167] op_sel_hi:[1,0]\n\t"
      "v_pk_mul_f32 v[30:31], v[30:31], v[166:167] op_sel_hi:[1,0]\n\t"
      "v_cvt_pk_bf16_f32 v80, v16, v17\n\t"
      "v_cvt_pk_bf16_f32 v81, v18, v19\n\t"
      "v_cvt_pk_bf16_f32 v82, v20, v21\n\t"
      "v_cvt_pk_bf16_f32 v83, v22, v23\n\t"
      "v_cvt_pk_bf16_f32 v84, v24, v25\n\t"
      "v_cvt_pk_bf16_f32 v85, v26, v27\n\t"
      "v_cvt_pk_bf16_f32 v86, v28, v29\n\t"
      "v_cvt_pk_bf16_f32 v87, v30, v31\n\t"
      "ds_write_b64 v240, v[80:81] offset:2560\n\t"
      "ds_write_b64 v240, v[82:83] offset:2576\n\t"
      "ds_write_b64 v240, v[84:85] offset:2592\n\t"
      "ds_write_b64 v240, v[86:87] offset:2608\n\t"
      "ds_read_b128 v[88:91], v241 offset:2560\n\t"
      "ds_read_b128 v[92:95], v241 offset:3840\n\t"
      "v_exp_f32 v128, v128\n\t"
      "v_exp_f32 v129, v129\n\t"
      "v_smfmac_f32_32x32x32_bf16 v[32:47], a[24:27], v[152:159], v224\n\t"
      "v_exp_f32 v130, v130\n\t"
      "v_exp_f32 v131, v131\n\t"
      "v_exp_f32 v132, v132\n\t"
      "v_smfmac_f32_16x16x64_bf16 v[216:219], a[32:35], v[152:159], v224\n\t"
      "v_mfma_f32_16x16x32_bf16 v[160:163], v[80:83], v[80:83], v[160:163]\n\t"
      "s_waitcnt lgkmcnt(8)\n\t"
      "ds_read_b64_tr_b16 a[28:29], v227 offset:3072\n\t"
      "v_exp_f32 v133, v133\n\t"
      "v_exp_f32 v134, v134\n\t"
      "v_mfma_f32_32x32x16_bf16 v[144:159], a[8:11], v[192:195], v[96:111]\n\t"
      "s_waitcnt lgkmcnt(1)\n\t"
      "v_exp_f32 v135, v135\n\t"
      "v_exp_f32 v136, v136\n\t"
      "v_exp_f32 v137, v137\n\t"
      "v_mfma_f32_32x32x16_bf16 v[144:159], a[12:15], v[196:199], v[144:159]\n\t"
      "global_store_dwordx4 v239, v[88:91], %[optr] offset:2048 nt\n\t"
      "v_exp_f32 v138, v138\n\t"
      "v_exp_f32 v139, v139\n\t"
      "v_exp_f32 v140, v140\n\t"
      "v_smfmac_f32_32x32x32_bf16 v[48:63], a[16:19], v[128:135], v224\n\t"
      "s_nop 0\n\t"
      "global_store_dwordx4 v239, v[92:95], %[optr] offset:3072 nt\n\t"
      "v_exp_f32 v141, v141\n\t"
      "v_exp_f32 v142, v142\n\t"
      "v_exp_f32 v143, v143\n\t"
      "v_smfmac_f32_16x16x64_bf16 v[220:223], a[32:35], v[128:135], v224\n\t"
      "v_mfma_f32_16x16x32_bf16 v[160:163], v[84:87], v[84:87], v[160:163]\n\t"
      "ds_read_b64_tr_b16 a[30:31], v227 offset:3328\n\t"
      "v_exp_f32 v144, v144\n\t"
      "v_exp_f32 v145, v145\n\t"
      "v_smfmac_f32_32x32x32_bf16 v[48:63], a[24:27], v[136:143], v224\n\t"
      "ds_read_b128 a[0:3], v225 offset:4096\n\t"
      "v_exp_f32 v146, v146\n\t"
      "v_exp_f32 v147, v147\n\t"
      "v_exp_f32 v148, v148\n\t"
      "v_smfmac_f32_16x16x64_bf16 v[220:223], a[32:35], v[136:143], v224\n\t"
      "ds_read_b128 a[4:7], v226 offset:4096\n\t"
      "v_exp_f32 v149, v149\n\t"
      "v_exp_f32 v150, v150\n\t"
      "v_mfma_f32_32x32x16_bf16 v[128:143], a[8:11], v[200:203], v[112:127]\n\t"
      "v_exp_f32 v151, v151\n\t"
      "v_exp_f32 v152, v152\n\t"
      "v_exp_f32 v153, v153\n\t"
      "v_mfma_f32_32x32x16_bf16 v[128:143], a[12:15], v[204:207], v[128:143]\n\t"
      "s_waitcnt lgkmcnt(10)\n\t"
      "v_exp_f32 v154, v154\n\t"
      "v_exp_f32 v155, v155\n\t"
      "v_exp_f32 v156, v156\n\t"
      "v_smfmac_f32_32x32x32_bf16 v[32:47], a[20:23], v[144:151], v224\n\t"
      "s_nop 1\n\t"
      "v_exp_f32 v157, v157\n\t"
      "v_exp_f32 v158, v158\n\t"
      "v_exp_f32 v159, v159\n\t"
      "v_smfmac_f32_16x16x64_bf16 v[216:219], a[32:35], v[144:151], v224\n\t"
      "s_waitcnt lgkmcnt(2)\n\t"
      "ds_read_b64_tr_b16 a[16:17], v227 offset:4096\n\t"
      "v_exp_f32 v128, v128\n\t"
      "v_exp_f32 v129, v129\n\t"
      "v_smfmac_f32_32x32x32_bf16 v[32:47], a[28:31], v[152:159], v224\n\t"
      "v_exp_f32 v130, v130\n\t"
      "v_exp_f32 v131, v131\n\t"
      "v_exp_f32 v132, v132\n\t"
      "v_smfmac_f32_16x16x64_bf16 v[216:219], a[32:35], v[152:159], v224\n\t"
      "s_waitcnt lgkmcnt(1)\n\t"
      "ds_read_b64_tr_b16 a[18:19], v227 offset:4352\n\t"
      "v_exp_f32 v133, v133\n\t"
      "v_exp_f32 v134, v134\n\t"
      "v_mfma_f32_32x32x16_bf16 v[144:159], a[0:3], v[200:203], v[112:127]\n\t"
      "v_exp_f32 v135, v135\n\t"
      "v_exp_f32 v136, v136\n\t"
      "v_exp_f32 v137, v137\n\t"
      "v_mfma_f32_32x32x16_bf16 v[144:159], a[4:7], v[204:207], v[144:159]\n\t"
      "s_nop 0\n\t"
      "v_exp_f32 v138, v138\n\t"
      "v_exp_f32 v139, v139\n\t"
      "v_exp_f32 v140, v140\n\t"
      "v_smfmac_f32_32x32x32_bf16 v[48:63], a[20:23], v[128:135], v224\n\t"
      "s_nop 0\n\t"
      "ds_read_b64_tr_b16 a[24:25], v227 offset:5120\n\t"
      "v_exp_f32 v141, v141\n\t"
      "v_exp_f32 v142, v142\n\t"
      "v_exp_f32 v143, v143\n\t"
      "v_smfmac_f32_16x16x64_bf16 v[220:223], a[32:35], v[128:135], v224\n\t"
      "ds_read_b64_tr_b16 a[26:27], v227 offset:5376\n\t"
      "v_mov_b32 v217, v216\n\t"
      "s_nop 1\n\t"
      "v_permlane32_swap_b32 v216, v217\n\t"
      "v_rcp_f32 v168, v216\n\t"
      "s_nop 0\n\t"
      "v_pk_mul_f32 v[32:33], v[32:33], v[168:169] op_sel_hi:[1,0]\n\t"
      "v_pk_mul_f32 v[34:35], v[34:35], v[168:169] op_sel_hi:[1,0]\n\t"
      "v_pk_mul_f32 v[36:37], v[36:37], v[168:169] op_sel_hi:[1,0]\n\t"
      "v_pk_mul_f32 v[38:39], v[38:39], v[168:169] op_sel_hi:[1,0]\n\t"
      "v_pk_mul_f32 v[40:41], v[40:41], v[168:169] op_sel_hi:[1,0]\n\t"
      "v_pk_mul_f32 v[42:43], v[42:43], v[168:169] op_sel_hi:[1,0]\n\t"
      "v_pk_mul_f32 v[44:45], v[44:45], v[168:169] op_sel_hi:[1,0]\n\t"
      "v_pk_mul_f32 v[46:47], v[46:47], v[168:169] op_sel_hi:[1,0]\n\t"
      "v_cvt_pk_bf16_f32 v96, v32, v33\n\t"
      "v_cvt_pk_bf16_f32 v97, v34, v35\n\t"
      "v_cvt_pk_bf16_f32 v98, v36, v37\n\t"
      "v_cvt_pk_bf16_f32 v99, v38, v39\n\t"
      "v_cvt_pk_bf16_f32 v100, v40, v41\n\t"
      "v_cvt_pk_bf16_f32 v101, v42, v43\n\t"
      "v_cvt_pk_bf16_f32 v102, v44, v45\n\t"
      "v_cvt_pk_bf16_f32 v103, v46, v47\n\t"
      "ds_write_b64 v240, v[96:97] offset:5120\n\t"
      "ds_write_b64 v240, v[98:99] offset:5136\n\t"
      "ds_write_b64 v240, v[100:101] offset:5152\n\t"
      "ds_write_b64 v240, v[102:103] offset:5168\n\t"
      "ds_read_b128 v[104:107], v241 offset:5120\n\t"
      "ds_read_b128 v[108:111], v241 offset:6400\n\t"
      "v_exp_f32 v144, v144\n\t"
      "v_exp_f32 v145, v145\n\t"
      "v_smfmac_f32_32x32x32_bf16 v[48:63], a[28:31], v[136:143], v224\n\t"
      "v_exp_f32 v146, v146\n\t"
      "v_exp_f32 v147, v147\n\t"
      "v_exp_f32 v148, v148\n\t"
      "v_smfmac_f32_16x16x64_bf16 v[220:223], a[32:35], v[136:143], v224\n\t"
      "v_mfma_f32_16x16x32_bf16 v[160:163], v[96:99], v[96:99], v[160:163]\n\t"
      "v_exp_f32 v149, v149\n\t"
      "v_exp_f32 v150, v150\n\t"
      "s_waitcnt lgkmcnt(0)\n\t"
      "v_exp_f32 v151, v151\n\t"
      "v_exp_f32 v152, v152\n\t"
      "v_exp_f32 v153, v153\n\t"
      "s_waitcnt lgkmcnt(8)\n\t"
      "global_store_dwordx4 v234, v[104:107], %[optr] offset:0 nt\n\t"
      "v_exp_f32 v154, v154\n\t"
      "v_exp_f32 v155, v155\n\t"
      "v_exp_f32 v156, v156\n\t"
      "v_smfmac_f32_32x32x32_bf16 v[48:63], a[16:19], v[144:151], v224\n\t"
      "s_nop 0\n\t"
      "global_store_dwordx4 v234, v[108:111], %[optr] offset:1024 nt\n\t"
      "v_exp_f32 v157, v157\n\t"
      "v_exp_f32 v158, v158\n\t"
      "v_exp_f32 v159, v159\n\t"
      "v_smfmac_f32_16x16x64_bf16 v[220:223], a[32:35], v[144:151], v224\n\t"
      "v_mfma_f32_16x16x32_bf16 v[160:163], v[100:103], v[100:103], v[160:163]\n\t"
      "s_waitcnt lgkmcnt(6)\n\t"
      "v_smfmac_f32_32x32x32_bf16 v[48:63], a[24:27], v[152:159], v224\n\t"
      "v_smfmac_f32_16x16x64_bf16 v[220:223], a[32:35], v[152:159], v224\n\t"
      "s_nop 7\n\t"
      "v_mov_b32 v221, v220\n\t"
      "s_nop 1\n\t"
      "v_permlane32_swap_b32 v220, v221\n\t"
      "v_rcp_f32 v170, v220\n\t"
      "s_nop 0\n\t"
      "v_pk_mul_f32 v[48:49], v[48:49], v[170:171] op_sel_hi:[1,0]\n\t"
      "v_pk_mul_f32 v[50:51], v[50:51], v[170:171] op_sel_hi:[1,0]\n\t"
      "v_pk_mul_f32 v[52:53], v[52:53], v[170:171] op_sel_hi:[1,0]\n\t"
      "v_pk_mul_f32 v[54:55], v[54:55], v[170:171] op_sel_hi:[1,0]\n\t"
      "v_pk_mul_f32 v[56:57], v[56:57], v[170:171] op_sel_hi:[1,0]\n\t"
      "v_pk_mul_f32 v[58:59], v[58:59], v[170:171] op_sel_hi:[1,0]\n\t"
      "v_pk_mul_f32 v[60:61], v[60:61], v[170:171] op_sel_hi:[1,0]\n\t"
      "v_pk_mul_f32 v[62:63], v[62:63], v[170:171] op_sel_hi:[1,0]\n\t"
      "v_cvt_pk_bf16_f32 v112, v48, v49\n\t"
      "v_cvt_pk_bf16_f32 v113, v50, v51\n\t"
      "v_cvt_pk_bf16_f32 v114, v52, v53\n\t"
      "v_cvt_pk_bf16_f32 v115, v54, v55\n\t"
      "v_cvt_pk_bf16_f32 v116, v56, v57\n\t"
      "v_cvt_pk_bf16_f32 v117, v58, v59\n\t"
      "v_cvt_pk_bf16_f32 v118, v60, v61\n\t"
      "v_cvt_pk_bf16_f32 v119, v62, v63\n\t"
      "ds_write_b64 v240, v[112:113] offset:7680\n\t"
      "ds_write_b64 v240, v[114:115] offset:7696\n\t"
      "ds_write_b64 v240, v[116:117] offset:7712\n\t"
      "ds_write_b64 v240, v[118:119] offset:7728\n\t"
      "ds_read_b128 v[120:123], v241 offset:7680\n\t"
      "ds_read_b128 v[124:127], v241 offset:8960\n\t"
      "v_mfma_f32_16x16x32_bf16 v[160:163], v[112:115], v[112:115], v[160:163]\n\t"
      "v_mfma_f32_16x16x32_bf16 v[160:163], v[116:119], v[116:119], v[160:163]\n\t"
      "s_waitcnt lgkmcnt(0)\n\t"
      "global_store_dwordx4 v234, v[120:123], %[optr] offset:2048 nt\n\t"
      "global_store_dwordx4 v234, v[124:127], %[optr] offset:3072 nt\n\t"
      "s_nop 7\n\t"
      : "={v208}"(l0), "={v212}"(l1), "={v216}"(l2), "={v220}"(l3),
        "+{v225}"(kp0), "+{v226}"(kp1), "+{v227}"(vp), "+{v235}"(t0), "+{v236}"(t1), "+{v[160:163]}"(ones),
        "+{v[64:67]}"(ra0), "+{v[68:71]}"(ra1), "+{v[80:83]}"(rb0), "+{v[84:87]}"(rb1)
      : "{v224}"(sidx), "{v228}"(koff), "{v229}"(voff), "{v230}"(kb0), "{v231}"(kb1), "{v232}"(vb), "{v233}"(qoff), "{v239}"(ooff), "{v240}"(ew), "{v241}"(er),
        [qptr] "s"(Qw), [kptr] "s"(Kh), [vptr] "s"(Vh), [optr] "s"(Ow), [dst0] "s"(dst0), [dst1] "s"(dst1),
        [kpl] "s"((unsigned)(uintptr_t)Kh), [kph] "s"((unsigned)((uintptr_t)Kh >> 32)), [vpl] "s"((unsigned)(uintptr_t)Vh), [vph] "s"((unsigned)((uintptr_t)Vh >> 32))
      : "v0", "v1", "v2", "v3", "v4", "v5", "v6", "v7", "v8", "v9", "v10", "v11", "v12", "v13", "v14", "v15", "v16", "v17", "v18", "v19", "v20", "v21", "v22", "v23", "v24", "v25", "v26", "v27", "v28", "v29", "v30", "v31", "v32", "v33", "v34", "v35", "v36", "v37", "v38", "v39", "v40", "v41", "v42", "v43", "v44", "v45", "v46", "v47", "v48", "v49", "v50", "v51", "v52", "v53", "v54", "v55", "v56", "v57", "v58", "v59", "v60", "v61", "v62", "v63", "v72", "v73", "v74", "v75", "v76", "v77", "v78", "v79", "v88", "v89", "v90", "v91", "v92", "v93", "v94", "v95", "v96", "v97", "v98", "v99", "v100", "v101", "v102", "v103", "v104", "v105", "v106", "v107", "v108", "v109", "v110", "v111", "v112", "v113", "v114", "v115", "v116", "v117", "v118", "v119", "v120", "v121", "v122", "v123", "v124", "v125", "v126", "v127", "v128", "v129", "v130", "v131", "v132", "v133", "v134", "v135", "v136", "v137", "v138", "v139", "v140", "v141", "v142", "v143", "v144", "v145", "v146", "v147", "v148", "v149", "v150", "v151", "v152", "v153", "v154", "v155", "v156", "v157", "v158", "v159", "v164", "v165", "v166", "v167", "v168", "v169", "v170", "v171", "v172", "v173", "v174", "v175", "v176", "v177", "v178", "v179", "v180", "v181", "v182", "v183", "v184", "v185", "v186", "v187", "v188", "v189", "v190", "v191", "v192", "v193", "v194", "v195", "v196", "v197", "v198", "v199", "v200", "v201", "v202", "v203", "v204", "v205", "v206", "v207", "v209", "v210", "v211", "v213", "v214", "v215", "v217", "v218", "v219", "v221", "v222", "v223", "v234", "v237", "v238", "v242", "a0", "a1", "a2", "a3", "a4", "a5", "a6", "a7", "a8", "a9", "a10", "a11", "a12", "a13", "a14", "a15", "a16", "a17", "a18", "a19", "a20", "a21", "a22", "a23", "a24", "a25", "a26", "a27", "a28", "a29", "a30", "a31", "a32", "a33", "a34", "a35", "s60", "s61", "s62", "s63", "s64", "s65", "s66", "s67", "s68", "s69", "m0", "scc", "memory");
  const float probe = (fabsf(__uint_as_float(ones[0])) + fabsf(__uint_as_float(ones[1]))) + (fabsf(__uint_as_float(ones[2])) + fabsf(__uint_as_float(ones[3])));
  const bool ok = ((l0 + l1) + (l2 + l3) <= 1.0e30f) && (fminf(fminf(l0, l1), fminf(l2, l3)) >= 0.25f) && (probe <= 3.0e38f);
  if (__builtin_expect(!__all(ok), 0)) {
    asm volatile("s_waitcnt vmcnt(0)" ::: "memory");
#pragma unroll 1
    for (int u = 0; u < 4; ++u) slow_tile(Qw + (u * 32 + r32) * DH, Kh, Vh, Ow + (u * 32 + r32) * DH, lane);
  }
}

extern "C" void kernel_launch(void* const* d_in, const int* in_sizes, int n_in, void* d_out, int out_size, void* d_ws, size_t ws_size, hipStream_t stream) {
  (void)in_sizes; (void)n_in; (void)out_size; (void)d_ws; (void)ws_size;
  (void)hipFuncSetAttribute((const void*)attn_d32_kernel, hipFuncAttributeMaxDynamicSharedMemorySize, LDS_BYTES);
  hipLaunchKernelGGL(attn_d32_kernel, dim3(NBH * (SEQ / QWG)), dim3(NW * 64), LDS_BYTES, stream,
                     (const unsigned short*)d_in[0], (const unsigned short*)d_in[1], (const unsigned short*)d_in[2], (unsigned short*)d_out
                     );
}
```
